```python
import torch

def _softmax(x, axis=-1):
    return torch.softmax(x, dim=axis)
def _gelu(x):
    return torch.nn.functional.gelu(x, approximate="tanh")
def _silu(x):
    return torch.nn.functional.silu(x)
def _sigmoid(x):
    return torch.sigmoid(x)
def _layernorm(x, axis=-1, eps=1e-5):
    m = x.mean(dim=axis, keepdim=True)
    v = x.var(dim=axis, keepdim=True, unbiased=False)
    return (x - m) / torch.sqrt(v + eps)
def _scatter_add(tgt, idx, src, axis=0):
    return tgt.clone().index_add_(axis, idx, src)
def _segment_sum(src, seg, n):
    out = torch.zeros((n,) + src.shape[1:], dtype=src.dtype)
    return out.index_add_(0, seg, src)
def _pad(x, pw):
    flat = tuple(v for pair in reversed(pw) for v in pair)
    return torch.nn.functional.pad(x, flat)

kernel_name = 'interleave_layers__spectral_fnet__moe_arch_mod__prompt'

def setup_inputs():

    _w = (lambda a: (lambda t: t.to(torch.bfloat16) if t.is_floating_point() else t)(torch.as_tensor(a)))
    def _A_setup():
        B, S, D = 1, 2048, 512
        ns = torch.arange(S)[:, None] * torch.arange(S)[None, :]
        nd = torch.arange(D)[:, None] * torch.arange(D)[None, :]
        Ws_re = torch.cos(-2.0 * torch.pi * ns / S).to(float)
        Ws_im = torch.sin(-2.0 * torch.pi * ns / S).to(float)
        Wd_re = torch.cos(-2.0 * torch.pi * nd / D).to(float)
        Wd_im = torch.sin(-2.0 * torch.pi * nd / D).to(float)
        return {
            "x": _w((lambda *s: torch.randn(*s, dtype=torch.bfloat16) * 0.05)(B, S, D)),
            "dft_seq_re": _w(Ws_re),
            "dft_seq_im": _w(Ws_im),
            "dft_hid_re": _w(Wd_re),
            "dft_hid_im": _w(Wd_im),
        }
    _A_in = _A_setup()

    def _B_setup():
        B, S, D, F, C = 1, 2048, 512, 1792, 1
        return {
            "x": _w((lambda *s: torch.randn(*s, dtype=torch.bfloat16) * 0.05)(B, S, D)),
            "w_r": _w((lambda *s: torch.randn(*s, dtype=torch.bfloat16) * 1.6)(D)),
            "w_gate": _w((lambda *s: torch.randn(*s, dtype=torch.bfloat16) * 0.4)(D, F)),
            "w_up": _w((lambda *s: torch.randn(*s, dtype=torch.bfloat16) * 0.4)(D, F)),
            "w_down": _w((lambda *s: torch.randn(*s, dtype=torch.bfloat16) * 0.4)(F, D)),
        }
    _B_in = _B_setup()

    return {
        "A_x": _A_in["x"],
        "A_dft_seq_re": _A_in["dft_seq_re"],
        "A_dft_seq_im": _A_in["dft_seq_im"],
        "A_dft_hid_re": _A_in["dft_hid_re"],
        "A_dft_hid_im": _A_in["dft_hid_im"],
        "B_x": _B_in["x"],
        "B_w_r": _B_in["w_r"],
        "B_w_gate": _B_in["w_gate"],
        "B_w_up": _B_in["w_up"],
        "B_w_down": _B_in["w_down"],
    }

def reference(A_x, A_dft_seq_re, A_dft_seq_im, A_dft_hid_re, A_dft_hid_im, B_x, B_w_r, B_w_gate, B_w_up, B_w_down):
    def _A_ref(x, dft_seq_re, dft_seq_im, dft_hid_re, dft_hid_im):
        a = torch.einsum("bsd,ed->bse", x, dft_hid_re)
        b = torch.einsum("bsd,ed->bse", x, dft_hid_im)
        y = torch.einsum("ts,bse->bte", dft_seq_re, a) - torch.einsum("ts,bse->bte", dft_seq_im, b)
        return y

    def _B_ref(x, w_r, w_gate, w_up, w_down):
        B, S, D = x.shape
        C = 1
        r = x @ w_r
        idx = torch.argsort(-r, axis=-1)[:, :C]
        idx_e = (lambda x, n, axis: torch.repeat_interleave(x, n, dim=axis))(idx.reshape(B, C, 1), D, axis=2)
        x_sel = (lambda x, i, axis: torch.take_along_dim(x, i, dim=axis))(x, idx_e, axis=1)
        g = _silu(x_sel @ w_gate)
        u = x_sel @ w_up
        y = (g * u) @ w_down
        r_sel = _sigmoid((lambda x, i, axis: torch.take_along_dim(x, i, dim=axis))(r, idx, axis=1))
        y = y * r_sel.reshape(B, C, 1)
        b_off = (torch.arange(B) * S).reshape(B, 1)
        flat_idx = (idx + b_off).reshape(B * C)
        zero = (x * 0.0).reshape(B * S, D)
        upd = _scatter_add(zero, flat_idx, y.reshape(B * C, D), axis=0)
        return x + upd.reshape(B, S, D)

    x = A_x
    x = x + _A_ref(_layernorm(x, axis=-1), A_dft_seq_re, A_dft_seq_im, A_dft_hid_re, A_dft_hid_im)
    x = x + _B_ref(_layernorm(x, axis=-1), B_w_r, B_w_gate, B_w_up, B_w_down)
    x = x + _A_ref(_layernorm(x, axis=-1), A_dft_seq_re, A_dft_seq_im, A_dft_hid_re, A_dft_hid_im)
    x = x + _B_ref(_layernorm(x, axis=-1), B_w_r, B_w_gate, B_w_up, B_w_down)
    return x
```

```cpp
#include <hip/hip_runtime.h>
#include <hip/hip_bf16.h>
#include <stdint.h>

typedef short bf16x8 __attribute__((ext_vector_type(8)));
typedef float f32x4 __attribute__((ext_vector_type(4)));
typedef unsigned short u16;

constexpr int S = 2048, D = 512, F = 1792;

__device__ __forceinline__ float bf2f(u16 b) { return __builtin_bit_cast(float, (unsigned)b << 16); }
__device__ __forceinline__ u16 f2bf(float f) { __bf16 b = (__bf16)f; return __builtin_bit_cast(u16, b); }

__device__ __forceinline__ float wave_sum(float v) {
  for (int o = 32; o; o >>= 1) v += __shfl_xor(v, o);
  return v;
}

__global__ void k_ln(const u16* x, u16* z) {
  int row = blockIdx.x * 4 + (threadIdx.x >> 6), lane = threadIdx.x & 63;
  bf16x8 v = *(const bf16x8*)(x + row * D + lane * 8);
  float f[8], s = 0;
  for (int j = 0; j < 8; ++j) { f[j] = bf2f((u16)v[j]); s += f[j]; }
  float m = wave_sum(s) * (1.f / D), q = 0;
  for (int j = 0; j < 8; ++j) { f[j] -= m; q += f[j] * f[j]; }
  float rs = rsqrtf(wave_sum(q) * (1.f / D) + 1e-5f);
  bf16x8 o;
  for (int j = 0; j < 8; ++j) o[j] = (short)f2bf(f[j] * rs);
  *(bf16x8*)(z + row * D + lane * 8) = o;
}

__global__ void k_gemm(const u16* A, const u16* Bt, const u16* A2, const u16* Bt2, const u16* res, u16* C, int M, int N, int K) {
  int w = threadIdx.x >> 6, lane = threadIdx.x & 63;
  int m0 = blockIdx.y * 64 + (w >> 1) * 32, n0 = blockIdx.x * 64 + (w & 1) * 32;
  f32x4 acc[2][2] = {}, acc2[2][2] = {};
  int fr = lane & 15, fq = lane >> 4;
  for (int k = 0; k < K; k += 32) {
    bf16x8 a[2], b[2];
    for (int i = 0; i < 2; ++i) a[i] = *(const bf16x8*)(A + (size_t)(m0 + i * 16 + fr) * K + k + fq * 8);
    for (int i = 0; i < 2; ++i) b[i] = *(const bf16x8*)(Bt + (size_t)(n0 + i * 16 + fr) * K + k + fq * 8);
    for (int i = 0; i < 2; ++i) for (int j = 0; j < 2; ++j) acc[i][j] = __builtin_amdgcn_mfma_f32_16x16x32_bf16(a[i], b[j], acc[i][j], 0, 0, 0);
    if (A2) {
      for (int i = 0; i < 2; ++i) a[i] = *(const bf16x8*)(A2 + (size_t)(m0 + i * 16 + fr) * K + k + fq * 8);
      for (int i = 0; i < 2; ++i) b[i] = *(const bf16x8*)(Bt2 + (size_t)(n0 + i * 16 + fr) * K + k + fq * 8);
      for (int i = 0; i < 2; ++i) for (int j = 0; j < 2; ++j) acc2[i][j] = __builtin_amdgcn_mfma_f32_16x16x32_bf16(a[i], b[j], acc2[i][j], 0, 0, 0);
    }
  }
  for (int i = 0; i < 2; ++i) for (int j = 0; j < 2; ++j) for (int r = 0; r < 4; ++r) {
    int row = m0 + i * 16 + fq * 4 + r, col = n0 + j * 16 + fr;
    float v = acc[i][j][r];
    if (A2) v = bf2f(res[(size_t)row * N + col]) + (v - acc2[i][j][r]);
    C[(size_t)row * N + col] = f2bf(v);
  }
}

__global__ void k_ln_r(const u16* x, const u16* wr, u16* z, u16* xo, unsigned long long* key) {
  int row = blockIdx.x * 4 + (threadIdx.x >> 6), lane = threadIdx.x & 63;
  bf16x8 v = *(const bf16x8*)(x + row * D + lane * 8);
  bf16x8 wv = *(const bf16x8*)(wr + lane * 8);
  float f[8], g[8], s = 0;
  for (int j = 0; j < 8; ++j) { f[j] = bf2f((u16)v[j]); s += f[j]; }
  float m = wave_sum(s) * (1.f / D), q = 0;
  for (int j = 0; j < 8; ++j) { g[j] = f[j] - m; q += g[j] * g[j]; }
  float rs = rsqrtf(wave_sum(q) * (1.f / D) + 1e-5f);
  bf16x8 o, o2; float r = 0;
  for (int j = 0; j < 8; ++j) { u16 zb = f2bf(g[j] * rs); o[j] = (short)zb; r += bf2f(zb) * bf2f((u16)wv[j]); o2[j] = (short)f2bf(f[j] + bf2f(zb)); }
  r = wave_sum(r);
  *(bf16x8*)(z + row * D + lane * 8) = o;
  *(bf16x8*)(xo + row * D + lane * 8) = o2;
  if (lane == 0) {
    unsigned u = __builtin_bit_cast(unsigned, r);
    u = (u & 0x80000000u) ? ~u : (u | 0x80000000u);
    atomicMax(key, ((unsigned long long)u << 32) | (unsigned)(S - 1 - row));
  }
}

__global__ void k_ffn1(const u16* z, const unsigned long long* key, const u16* wg, const u16* wu, u16* h) {
  __shared__ float red[2][8][32];
  int idx = S - 1 - (int)(unsigned)(*key);
  int c = blockIdx.x * 32 + (threadIdx.x & 31), kg = threadIdx.x >> 5;
  float g = 0, u = 0;
  for (int k = kg * 64; k < kg * 64 + 64; ++k) { float zv = bf2f(z[idx * D + k]); g += zv * bf2f(wg[k * F + c]); u += zv * bf2f(wu[k * F + c]); }
  red[0][kg][threadIdx.x & 31] = g; red[1][kg][threadIdx.x & 31] = u;
  __syncthreads();
  if (threadIdx.x < 32) {
    g = 0; u = 0;
    for (int i = 0; i < 8; ++i) { g += red[0][i][threadIdx.x]; u += red[1][i][threadIdx.x]; }
    g = bf2f(f2bf(g)); u = bf2f(f2bf(u));
    float sg = bf2f(f2bf(g / (1.f + __expf(-g))));
    h[c] = f2bf(sg * u);
  }
}

__global__ void k_ffn2(const u16* x, const u16* z, const unsigned long long* key, const u16* h, const u16* wd, u16* xo) {
  __shared__ float red[16][16];
  unsigned long long kv = *key;
  int idx = S - 1 - (int)(unsigned)kv;
  unsigned u = (unsigned)(kv >> 32); u = (u & 0x80000000u) ? (u & 0x7fffffffu) : ~u;
  float r = bf2f(f2bf(__builtin_bit_cast(float, u)));
  int c = blockIdx.x * 16 + (threadIdx.x & 15), kg = threadIdx.x >> 4;
  float y = 0;
  for (int k = kg * 112; k < kg * 112 + 112; ++k) y += bf2f(h[k]) * bf2f(wd[k * D + c]);
  red[kg][threadIdx.x & 15] = y;
  __syncthreads();
  if (threadIdx.x < 16) {
    y = 0;
    for (int i = 0; i < 16; ++i) y += red[i][threadIdx.x];
    float sg = bf2f(f2bf(1.f / (1.f + __expf(-r))));
    y = bf2f(f2bf(bf2f(f2bf(y)) * sg));
    float zz = bf2f(f2bf(bf2f(z[idx * D + c]) + y));
    xo[idx * D + c] = f2bf(bf2f(x[idx * D + c]) + zz);
  }
}

extern "C" void kernel_launch(void* const* d_in, const int* in_sizes, int n_in, void* d_out, int out_size, void* d_ws, size_t ws_size, hipStream_t stream) {
  const u16* x = (const u16*)d_in[0];
  const u16* Cs = (const u16*)d_in[1]; const u16* Ss = (const u16*)d_in[2];
  const u16* Cd = (const u16*)d_in[3]; const u16* Sd = (const u16*)d_in[4];
  const u16* wr = (const u16*)d_in[6]; const u16* wg = (const u16*)d_in[7]; const u16* wu = (const u16*)d_in[8]; const u16* wd = (const u16*)d_in[9];
  char* ws = (char*)d_ws;
  unsigned long long* key = (unsigned long long*)ws;
  u16* z = (u16*)(ws + 4096); u16* aT = z + S * D; u16* bT = aT + S * D; u16* x1 = bT + S * D; u16* x2 = x1 + S * D; u16* h = x2 + S * D;
  u16* out = (u16*)d_out;
  hipMemsetAsync(key, 0, 64, stream);
  const u16* xin = x;
  for (int l = 0; l < 2; ++l) {
    u16* xo = l ? out : x2;
    k_ln<<<S / 4, 256, 0, stream>>>(xin, z);
    k_gemm<<<dim3(S / 64, D / 64), 256, 0, stream>>>(Cd, z, nullptr, nullptr, nullptr, aT, D, S, D);
    k_gemm<<<dim3(S / 64, D / 64), 256, 0, stream>>>(Sd, z, nullptr, nullptr, nullptr, bT, D, S, D);
    k_gemm<<<dim3(D / 64, S / 64), 256, 0, stream>>>(Cs, aT, Ss, bT, xin, x1, S, D, S);
    k_ln_r<<<S / 4, 256, 0, stream>>>(x1, wr, z, xo, key + l);
    k_ffn1<<<F / 32, 256, 0, stream>>>(z, key + l, wg, wu, h);
    k_ffn2<<<D / 16, 256, 0, stream>>>(x1, z, key + l, h, wd, xo);
    xin = xo;
  }
}
```

```cpp
#include <hip/hip_runtime.h>
#include <hip/hip_bf16.h>
#include <stdint.h>
#include <utility>

typedef unsigned u32;
typedef unsigned long long u64;
typedef unsigned short u16;
typedef u32 v4u __attribute__((ext_vector_type(4)));
typedef u32 v2u __attribute__((ext_vector_type(2)));
typedef float f2 __attribute__((ext_vector_type(2)));
typedef float f4 __attribute__((ext_vector_type(4)));
typedef __bf16 bf2 __attribute__((ext_vector_type(2)));
typedef __amdgpu_buffer_rsrc_t rsrc_t;
#define DI __device__ __forceinline__
#define LAS __attribute__((address_space(3)))
#define SB() __builtin_amdgcn_sched_barrier(0)

constexpr u32 SENT = 0xFFFFFFFFu;
constexpr int SC1 = 16;
constexpr int SPIN_MAX = 200000;
constexpr u32 O_EPOCH = 0, O_FLAG = 4096, O_SMALL = 0x40000;
constexpr u32 O_KEY1 = O_SMALL, O_H1 = O_SMALL + 16384, O_Y1 = O_SMALL + 49152, O_DEL = O_SMALL + 65536, O_KEY2 = O_SMALL + 98304, O_H2 = O_SMALL + 114688;
constexpr u32 SMALL_KB = 144;
constexpr u32 O_CAND = 0x80000, O_AB0 = 1u << 20, O_AB1 = 5u << 20, O_PQ0 = 9u << 20, O_PQ1 = 13u << 20, WS_BYTES = 17u << 20;
constexpr u32 O_DBG = 32u << 20;
constexpr u32 L_W = 0, L_RED = 131072, L_WRF = 155648, L_DX = 157696, L_P1 = 159744, L_P2 = 160000, L_ST = 160064, L_BC = 160576, LDS_BYTES = 163840;

DI rsrc_t mkrs(const void* p, u32 bytes) { return __builtin_amdgcn_make_buffer_rsrc((void*)p, 0, bytes, 0x00020000); }
DI v4u ld16(rsrc_t r, u32 v, u32 s) { return __builtin_amdgcn_raw_buffer_load_b128(r, v, s, 0); }
DI v4u ld16c(rsrc_t r, u32 v, u32 s) { return __builtin_amdgcn_raw_buffer_load_b128(r, v, s, SC1); }
DI u32 ld4(rsrc_t r, u32 v, u32 s) { return __builtin_amdgcn_raw_buffer_load_b32(r, v, s, 0); }
DI u32 ld4c(rsrc_t r, u32 v, u32 s) { return __builtin_amdgcn_raw_buffer_load_b32(r, v, s, SC1); }
DI void st16c(rsrc_t r, u32 v, u32 s, v4u d) { __builtin_amdgcn_raw_buffer_store_b128(d, r, v, s, SC1); }
DI void st8c(rsrc_t r, u32 v, u32 s, v2u d) { __builtin_amdgcn_raw_buffer_store_b64(d, r, v, s, SC1); }
DI void st4c(rsrc_t r, u32 v, u32 s, u32 d) { __builtin_amdgcn_raw_buffer_store_b32(d, r, v, s, SC1); }
DI float blo(u32 d) { return __builtin_bit_cast(float, d << 16); }
DI float bhi(u32 d) { return __builtin_bit_cast(float, d & 0xffff0000u); }
DI f2 unpk(u32 d) { return f2{blo(d), bhi(d)}; }
DI u32 pk(f2 v) { return __builtin_bit_cast(u32, __builtin_convertvector(v, bf2)); }
DI float rbf(float v) { return blo(pk(f2{v, 0.f})); }
DI u32 san(u32 d) { return d < SENT ? d : SENT - 1u; }
DI u32 fbits(float f) { return __builtin_bit_cast(u32, f); }
DI float bitsf(u32 u) { return __builtin_bit_cast(float, u); }
DI u32 max4(v4u a) { u32 m = a[0] > a[1] ? a[0] : a[1]; u32 n = a[2] > a[3] ? a[2] : a[3]; return m > n ? m : n; }
DI u32 umax(u32 a, u32 b) { return a > b ? a : b; }
template <int C> DI float dppf(float x) { return bitsf(__builtin_amdgcn_mov_dpp((int)fbits(x), C, 0xf, 0xf, true)); }
template <int C> DI u32 dppu(u32 x) { return (u32)__builtin_amdgcn_mov_dpp((int)x, C, 0xf, 0xf, true); }
DI float xsum16(float x) { auto s = __builtin_amdgcn_permlane16_swap(fbits(x), fbits(x), false, false); return bitsf(s[0]) + bitsf(s[1]); }
DI float xsum32(float x) { auto s = __builtin_amdgcn_permlane32_swap(fbits(x), fbits(x), false, false); return bitsf(s[0]) + bitsf(s[1]); }
DI float rowsum(float x) { x += dppf<0x128>(x); x += dppf<0x124>(x); x += dppf<0x122>(x); x += dppf<0x121>(x); return x; }
DI float wavesum(float x) { return xsum32(xsum16(rowsum(x))); }
DI u32 rowmax(u32 x) { x = umax(x, dppu<0x128>(x)); x = umax(x, dppu<0x124>(x)); x = umax(x, dppu<0x122>(x)); x = umax(x, dppu<0x121>(x)); return x; }
DI u32 wavemax(u32 x) {
  x = rowmax(x);
  auto s = __builtin_amdgcn_permlane16_swap(x, x, false, false); x = umax(s[0], s[1]);
  auto t = __builtin_amdgcn_permlane32_swap(x, x, false, false); return umax(t[0], t[1]);
}
DI u32 mkkey(float r, u32 row) { u32 u = fbits(r); u = (u & 0x80000000u) ? ~u : (u | 0x80000000u); return san((u & ~2047u) | (2047u - row)); }
DI u32 rfl(u32 x) { return __builtin_amdgcn_readfirstlane(x); }
DI u32 opq(u32 x) { asm volatile("" : "+v"(x)); return x; }
DI void bar() { asm volatile("s_waitcnt lgkmcnt(0)\n\ts_barrier" ::: "memory"); }
template <typename T> DI T ldl(u32 a, u32 o = 0) { return *(const LAS T*)((const LAS char*)(size_t)a + o); }
template <typename T> DI void stl(u32 a, u32 o, T v) { *(LAS T*)((LAS char*)(size_t)a + o) = v; }
#define MFMA0(acc, a, b, CA, CB) asm volatile("v_mfma_f32_16x16x32_bf16 %0, %1, %2, 0" : "=&v"(acc) : CA(a), CB(b))
#define MFMA(acc, a, b, CA, CB) asm volatile("v_mfma_f32_16x16x32_bf16 %0, %1, %2, %0" : "+v"(acc) : CA(a), CB(b))
#define VM(n) asm volatile("s_waitcnt vmcnt(%0)" :: "i"(n) : "memory")

template <int... I, typename F> DI void sfor_(std::integer_sequence<int, I...>, F&& f) { (f(std::integral_constant<int, I>{}), ...); }
template <int N, typename F> DI void sfor(F&& f) { sfor_(std::make_integer_sequence<int, N>{}, f); }

#define STAMP(n)

#define NA1 44

constexpr int cntA(int n) {
  const int v = n < 0 ? 0 : n < 26 ? n / 2 + 1 : 13 + (n - 25);
  return v < NA1 ? v : NA1;
}

struct Ctx {
  rsrc_t ws;
  u32 lane, l16, w, tid, i, c, cg, im, rep;
};

template <int L, typename HOOK>
DI void gemm1(const Ctx& k, f2 (&X)[16][4], float sc, float of, HOOK&& hook) {
  v4u Z[16], Wf[8];
  f4 acc[2][2];
  const u32 ln = opq(k.lane), t16 = ln & 15, q = ln >> 4, sq = ((q & 1) << 1) | (q >> 1);
  u32 wb[8];
#pragma unroll
  for (int h = 0; h < 8; ++h) wb[h] = opq(L_W + (h >> 2) * 65536 + t16 * 1024 + (((4 * (h & 3) + sq) ^ t16) << 4));
  const u32 abv = (ln >> 5) * 256 + (q & 1) * 1024 + t16 * 16;
  const u32 abs0 = (L ? O_AB1 : O_AB0) + k.c * 524288 + (2 * k.i + (k.w >> 1)) * 2048 + (k.w & 1) * 512;
  auto mkz = [&](int s) __attribute__((always_inline)) {
#pragma unroll
    for (int j = 0; j < 4; ++j) Z[s][j] = pk(X[s][j] * sc + of);
    asm volatile("" : "+v"(Z[s]));
  };
  auto ldw = [&](int n) __attribute__((always_inline)) {
    const int qd = n >> 4, s = n & 15;
#pragma unroll
    for (int j = 0; j < 2; ++j) { const int ct = 2 * qd + j; Wf[(2 * n + j) & 7] = ldl<v4u>(wb[(ct >> 2) * 4 + (s & 3)], (ct & 3) * 16384 + (s >> 2) * 256); }
  };
  auto fin = [&](int qd) __attribute__((always_inline)) {
    f4 a = acc[qd & 1][0], b = acc[qd & 1][1];
    u32 a0 = san(pk(f2{a[0], a[1]})), a1 = san(pk(f2{a[2], a[3]})), b0 = san(pk(f2{b[0], b[1]})), b1 = san(pk(f2{b[2], b[3]}));
    auto s0 = __builtin_amdgcn_permlane16_swap(a0, b0, false, false);
    auto s1 = __builtin_amdgcn_permlane16_swap(a1, b1, false, false);
    st16c(k.ws, abv, abs0 + qd * 131072, v4u{s0[0], s1[0], s0[1], s1[1]});
  };
#pragma unroll
  for (int n = 0; n < 4; ++n) ldw(n);
  mkz(0); mkz(1);
  asm volatile("s_nop 1" : "+v"(Z[0]), "+v"(Z[1]));
  SB();
  sfor<64>([&](auto ic) __attribute__((always_inline)) {
    constexpr int n = ic, qd = n >> 4, s = n & 15;
    if constexpr (s == 0) { MFMA0(acc[qd & 1][0], Z[s], Wf[(2 * n) & 7], "v", "v"); MFMA0(acc[qd & 1][1], Z[s], Wf[(2 * n + 1) & 7], "v", "v"); }
    else { MFMA(acc[qd & 1][0], Z[s], Wf[(2 * n) & 7], "v", "v"); MFMA(acc[qd & 1][1], Z[s], Wf[(2 * n + 1) & 7], "v", "v"); }
    SB();
    if constexpr (n + 4 < 64) ldw(n + 4);
    if constexpr (n + 2 < 16) mkz(n + 2);
    if constexpr (qd > 0 && s == 4) fin(qd - 1);
    hook(ic);
    SB();
  });
  asm volatile("s_nop 7" : "+v"(acc[1][0]), "+v"(acc[1][1]));
  fin(3);
}

template <int L> DI v4u ldb(const Ctx& k, u32 sb, int n) { return ld16c(k.ws, k.l16 + (n & 3) * 1024, sb + (n >> 5) * 131072 + ((n & 31) >> 2) * 4096); }

template <int L> DI void slowpass(const Ctx& k, u32 sb, int qd, const v4u (&A)[64], f4 (&acc)[4][2]) {
#pragma unroll
  for (int n = 0; n < 32; ++n) {
    v4u b;
    for (int sp = 0; sp < SPIN_MAX; ++sp) {
      b = ldb<L>(k, sb, qd * 32 + n);
      asm volatile("" ::: "memory");
      if (!__any(max4(b) == SENT)) break;
    }
#pragma unroll
    for (int T = 0; T < 4; ++T) { if (n < 2) MFMA0(acc[T][n & 1], b, A[T * 16 + (n >> 1)], "v", "a"); else MFMA(acc[T][n & 1], b, A[T * 16 + (n >> 1)], "v", "a"); }
    SB();
  }
  asm volatile("s_nop 7" : "+v"(acc[0][0]), "+v"(acc[0][1]), "+v"(acc[1][0]), "+v"(acc[1][1]), "+v"(acc[2][0]), "+v"(acc[2][1]), "+v"(acc[3][0]), "+v"(acc[3][1]));
}

template <int L, typename HOOK>
DI void gemm2(const Ctx& k, const v4u (&A)[64], v4u (&B)[16], u32 sb, const v4u (&xres)[4], HOOK&& hook) {
  f4 acc[2][4][2], rd[3][2];
  const u32 ln = opq(k.lane), q = ln >> 4, t = ln & 15, sq = ((q & 1) << 1) | (q >> 1);
  const u32 pv = L ? (t >> 3) * 16384 + sq * 128 + (t & 7) * 16 : k.l16;
  const u32 ps = L ? O_PQ1 + (k.i * 8 + 2 * k.w) * 16384 + k.im * 8192 + k.cg * 2048 : O_PQ0 + ((k.i * 4 + k.w) * 2 + k.im) * 16384 + k.cg * 4096;
  auto check = [&](int qd) __attribute__((always_inline)) {
    if (L == 1) {
      f4 s4 = acc[qd & 1][0][0] + acc[qd & 1][0][1];
      float s = (s4[0] + s4[1]) + (s4[2] + s4[3]);
      if (__builtin_expect(__any(s != s), 0)) slowpass<L>(k, sb, qd, A, acc[qd & 1]);
    }
  };
  auto wr = [&](int qd, int T) __attribute__((always_inline)) {
    const u32 a = opq(k.l16 + (L_RED + ((T + k.w) & 3) * 6144 + (T - 1) * 2048));
    stl<f4>(a, 0, acc[qd & 1][T][0]); stl<f4>(a, 1024, acc[qd & 1][T][1]);
  };
  auto rdl = [&](int u) __attribute__((always_inline)) { const u32 a = opq(k.l16 + (L_RED + k.w * 6144 + u * 2048)); rd[u][0] = ldl<f4>(a); rd[u][1] = ldl<f4>(a, 1024); };
  auto fin = [&](int qd) __attribute__((always_inline)) {
    f4 a = (acc[qd & 1][0][0] + rd[0][0]) + (rd[1][0] + rd[2][0]), b = (acc[qd & 1][0][1] + rd[0][1]) + (rd[1][1] + rd[2][1]);
    float v[8];
#pragma unroll
    for (int j = 0; j < 4; ++j) { auto s = __builtin_amdgcn_permlane16_swap(fbits(a[j]), fbits(b[j]), false, false); v[j] = bitsf(s[0]); v[4 + j] = bitsf(s[1]); }
    v4u o;
#pragma unroll
    for (int j = 0; j < 4; ++j) o[j] = san(pk(f2{v[2 * j], v[2 * j + 1]} + unpk(xres[qd][j])));
    st16c(k.ws, pv, ps + qd * (L ? 512 : 1024), o);
  };
  sfor<128>([&](auto ic) __attribute__((always_inline)) {
    constexpr int n = ic, qd = n >> 5, m = n & 31;
#pragma unroll
    for (int T = 0; T < 4; ++T) {
      if constexpr (m < 2) MFMA0(acc[qd & 1][T][m & 1], B[n & 15], A[T * 16 + (m >> 1)], "v", "a");
      else MFMA(acc[qd & 1][T][m & 1], B[n & 15], A[T * 16 + (m >> 1)], "v", "a");
    }
    SB();
    if constexpr (n + 16 < 128) B[n & 15] = ldb<L>(k, sb, n + 16);
    if constexpr (qd > 0) {
      if constexpr (m == 1) check(qd - 1);
      if constexpr (m >= 2 && m <= 4) wr(qd - 1, m - 1);
      if constexpr (m == 6) bar();
      if constexpr (m >= 7 && m <= 9) rdl(m - 7);
      if constexpr (m == 12) fin(qd - 1);
    }
    if constexpr (m == 26) bar();
    hook(ic);
    SB();
  });
  asm volatile("s_nop 7" : "+v"(acc[1][0][0]), "+v"(acc[1][0][1]), "+v"(acc[1][1][0]), "+v"(acc[1][1][1]), "+v"(acc[1][2][0]), "+v"(acc[1][2][1]), "+v"(acc[1][3][0]), "+v"(acc[1][3][1]));
  check(3);
  wr(3, 1); wr(3, 2); wr(3, 3);
  bar();
  rdl(0); rdl(1); rdl(2);
  fin(3);
}

DI v4u poll16(rsrc_t r, u32 v, u32 s, bool act = true) {
  v4u b;
  for (int sp = 0; sp < SPIN_MAX; ++sp) { b = ld16c(r, v, s); asm volatile("" ::: "memory"); if (!__any(act && max4(b) == SENT)) break; }
  return b;
}
DI u32 poll4(rsrc_t r, u32 v, u32 s) {
  u32 b;
  for (int sp = 0; sp < SPIN_MAX; ++sp) { b = ld4c(r, v, s); asm volatile("" ::: "memory"); if (!__any(b == SENT)) break; }
  return b;
}

DI void gemv1_part(const Ctx& k, float z0, float z1, const v4u (&GU)[4]) {
  float V[16];
#pragma unroll
  for (int h = 0; h < 2; ++h)
#pragma unroll
    for (int j = 0; j < 4; ++j) { f2 r = unpk(GU[2 * h][j]) * z0 + unpk(GU[2 * h + 1][j]) * z1; V[8 * h + 2 * j] = r[0]; V[8 * h + 2 * j + 1] = r[1]; }
  float S[8], T[4];
#pragma unroll
  for (int j = 0; j < 8; ++j) { auto s = __builtin_amdgcn_permlane32_swap(fbits(V[j]), fbits(V[j + 8]), false, false); S[j] = bitsf(s[0]) + bitsf(s[1]); }
#pragma unroll
  for (int j = 0; j < 4; ++j) { auto s = __builtin_amdgcn_permlane16_swap(fbits(S[j]), fbits(S[j + 4]), false, false); T[j] = rowsum(bitsf(s[0]) + bitsf(s[1])); }
  if ((k.lane & 15) == 0) stl<f4>(opq(L_P1 + k.w * 64 + (k.lane >> 4) * 16), 0, f4{T[0], T[1], T[2], T[3]});
}
DI void gemv1_fin(const Ctx& k, u32 oh, u32 jA, bool valid) {
  const u32 p = k.lane & 3, pa = opq(L_P1 + p * 8);
  f2 g = {0.f, 0.f}, u = {0.f, 0.f};
#pragma unroll
  for (int w = 0; w < 4; ++w) { g += ldl<f2>(pa, w * 64); u += ldl<f2>(pa, w * 64 + 32); }
  f2 h;
#pragma unroll
  for (int j = 0; j < 2; ++j) { const float gg = rbf(g[j]); h[j] = rbf(gg / (1.f + __expf(-gg))) * rbf(u[j]); }
  if (valid && k.lane < 32) st4c(k.ws, (k.lane >> 2) * 4096 + p * 4, oh + jA * 16, san(pk(h)));
}
DI void gemv2_part(const Ctx& k, v4u h, const u32 (&Dw)[8]) {
  f2 y = {0.f, 0.f};
#pragma unroll
  for (int j = 0; j < 4; ++j) { y += unpk(Dw[2 * j]) * blo(h[j]); y += unpk(Dw[2 * j + 1]) * bhi(h[j]); }
  if (k.tid >= 224) y = f2{0.f, 0.f};
  auto s = __builtin_amdgcn_permlane32_swap(fbits(y[0]), fbits(y[1]), false, false);
  const float v = rowsum(xsum16(bitsf(s[0]) + bitsf(s[1])));
  if ((k.lane & 31) == 0) stl<float>(opq(L_P2 + k.w * 8 + (k.lane >> 5) * 4), 0, v);
}
DI f2 gemv2_fin(float r) {
  const u32 pa = opq(L_P2);
  f2 y = (ldl<f2>(pa) + ldl<f2>(pa, 8)) + (ldl<f2>(pa, 16) + ldl<f2>(pa, 24));
  const float sg = rbf(1.f / (1.f + __expf(-rbf(r))));
  return f2{rbf(rbf(y[0]) * sg), rbf(rbf(y[1]) * sg)};
}

__global__ __launch_bounds__(256, 1) void fnet_mod_kernel(const u16* x, const u16* Cs, const u16* Ss, const u16* Cd, const u16* Sd, const u16* wr,
                                                          const u16* wg, const u16* wu, const u16* wd, u16* out, unsigned char* wsp) {
  extern __shared__ __attribute__((aligned(16))) unsigned char smem[];
  STAMP(0);
  Ctx k;
  k.tid = threadIdx.x; k.lane = k.tid & 63; k.w = rfl(k.tid >> 6);
  const u32 b = blockIdx.x, x8 = b & 7, r5 = b >> 3;
  k.c = (x8 & 1) * 4 + (r5 & 3); k.i = (x8 >> 1) * 8 + (r5 >> 2); k.cg = k.c & 3; k.im = k.c >> 2; k.rep = x8;
  k.ws = mkrs(wsp, WS_BYTES);
  k.l16 = opq(k.lane * 16);
  const u32 lane = k.lane, w = k.w, tid = k.tid, i = k.i, c = k.c, cg = k.cg, im = k.im, me = i * 8 + c;
  const u32 q = lane >> 4, t16 = lane & 15, sq = ((q & 1) << 1) | (q >> 1);
  asm volatile("" :: "v"((u32)(size_t)smem));

  u32 pfd, pfe; v2u ep;
  {
    u64 pc; u32 len;
    asm volatile("s_getpc_b64 %0\nfm_code_begin:\n\ts_mov_b32 %1, fm_code_end-fm_code_begin" : "=s"(pc), "=s"(len));
    u32 o = tid * 128, o2 = o + 32768; o = o < len ? o : 0; o2 = o2 < len ? o2 : 0;
    asm volatile("global_load_dword %0, %1, %2" : "=v"(pfd) : "v"(o), "s"(pc) : "memory");
    asm volatile("global_load_dword %0, %1, %2" : "=v"(pfe) : "v"(o2), "s"(pc) : "memory");
    asm volatile("buffer_load_dwordx2 %0, %1, %2, 0 offen sc1" : "=v"(ep) : "v"(0u), "s"(k.ws) : "memory");
  }
  v4u XR[16];
  const rsrc_t rx = mkrs(x, 2048 * 1024);
  {
    const u32 v = (64 * i + 16 * w + (lane >> 2)) * 1024 + (lane & 3) * 16;
#pragma unroll
    for (int s = 0; s < 16; ++s)
      asm volatile("buffer_load_dwordx4 %0, %1, %2, %3 offen offset:%4" : "=v"(XR[s]) : "v"(v), "s"(rx), "s"((((s >> 2) + cg) & 3) * 256), "i"((s & 3) * 64) : "memory");
  }
  const rsrc_t rw = mkrs(im ? Sd : Cd, 512 * 1024);
  const u32 wg0 = (((lane >> 4) + cg) & 3) * 256;
  auto ldW = [&](int n) __attribute__((always_inline)) {
    const u32 e = (n >> 3) * 32 + w * 8 + (n & 7);
    const u32 v = wg0 + (((lane ^ e) & 15) << 4);
    asm volatile("s_mov_b32 m0, %0\n\ts_nop 0\n\tbuffer_load_dwordx4 %1, %2, %3 offen lds" :: "s"(L_W + e * 1024), "v"(v), "s"(rw), "s"((128 * cg + e) * 1024) : "memory");
  };
#pragma unroll
  for (int n = 0; n < 16; ++n) ldW(n);
  v4u A[64];
  const rsrc_t rm = mkrs(im ? Ss : Cs, 2048 * 4096);
  const u32 av = (lane >> 2) * 4096 + (lane & 3) * 16;
  u32 as[4];
#pragma unroll
  for (int T = 0; T < 4; ++T) as[T] = (64 * i + 16 * ((T + w) & 3)) * 4096 + w * 1024;
#define LDA(n) asm volatile("buffer_load_dwordx4 %0, %1, %2, %3 offen offset:%4" : "=a"(A[n]) : "v"(av), "s"(rm), "s"(as[(n) >> 4]), "i"(((n) & 15) * 64) : "memory")
  SB();
  const u32 stw = opq(L_RED + w * 6144 + (lane >> 2) * 64 + (((lane & 3) ^ ((lane >> 5) * 3)) << 4));
  const u32 strd = L_RED + w * 6144 + t16 * 64;
  const u32 gx = (t16 >> 3) * 3;

  const u32 xrd = opq(strd + ((sq ^ gx) << 4));
  f2 X[16][4];
  v4u xres[4];
  const u32 rmask = im - 1u;
  f2 s1 = {0.f, 0.f}, s2 = {0.f, 0.f};
  v4u F[16];
#pragma unroll
  for (int s = 0; s < 18; ++s) {
    if (s < 16) {
      asm volatile("s_waitcnt vmcnt(31)" : "+v"(XR[s]) :: "memory");
      stl<v4u>(stw, (s % 6) * 1024, XR[s]);
      F[s] = ldl<v4u>(xrd, (s % 6) * 1024);
      ldW(16 + s);
      SB();
    }
    if (s >= 2) {
      const int u = s - 2;
      if (u < 4) xres[u] = F[u] & rmask;
#pragma unroll
      for (int j = 0; j < 4; ++j) { X[u][j] = unpk(F[u][j]); s1 += X[u][j]; s2 += X[u][j] * X[u][j]; }
      asm volatile("" : "+v"(X[u][0]), "+v"(X[u][1]), "+v"(X[u][2]), "+v"(X[u][3]), "+v"(s1), "+v"(s2));
      SB();
    }
  }
  STAMP(1);
  float mean = xsum32(xsum16(s1[0] + s1[1])) * (1.f / 512), var = xsum32(xsum16(s2[0] + s2[1])) * (1.f / 512) - mean * mean;
  float rstd = __builtin_amdgcn_rsqf(fmaxf(var, 0.f) + 1e-5f);
  VM(24);
  bar();
  STAMP(2);
  {
    const v4u sent = {SENT, SENT, SENT, SENT};
    gemm1<0>(k, X, rstd, -mean * rstd, [&](auto ic) __attribute__((always_inline)) {
      constexpr int n = ic;
      if constexpr (cntA(n) > cntA(n - 1)) LDA(cntA(n - 1));
      constexpr int u = (n & 1) ? n / 2 : -1;
      if constexpr (u >= 0 && u < 4) st16c(k.ws, k.l16, O_PQ0 + (me * 16 + w * 4 + u) * 1024, sent);
      if constexpr (u >= 4 && u < 8) st16c(k.ws, k.l16, O_AB1 + (c * 512 + i * 16 + w * 4 + (u - 4)) * 1024, sent);
      if constexpr (u >= 8 && u < 12) st16c(k.ws, k.l16, O_PQ1 + (me * 16 + w * 4 + (u - 8)) * 1024, sent);
      if constexpr (u == 12) st16c(k.ws, k.l16, w < 2 ? O_CAND + (me * 2 + w) * 1024 : (w == 2 && me < SMALL_KB) ? O_SMALL + me * 1024 : WS_BYTES, sent);
      if constexpr (n == 11 || n == 27 || n == 43) {
        constexpr int young = cntA(n) + ((n + 1) / 2 < 13 ? (n + 1) / 2 : 13) + (n / 16) - (n == 11 ? 0 : 0) + 8 * (2 - n / 16);
        asm volatile("s_waitcnt vmcnt(%0)\n\ts_barrier" :: "i"(young) : "memory");
      }
    });
  }
  STAMP(3);
  asm volatile("s_waitcnt vmcnt(0)" : "+v"(ep), "+v"(pfd), "+v"(pfe) :: "memory");
  bar();
  STAMP(4);
  const u64 epoch = ((u64)rfl(ep[1]) << 32) | rfl(ep[0]);
  const u64 tag64 = (epoch + 0x632BE59BD9B4E019ull) * 0x9E3779B97F4A7C15ull;
  const v2u tag = {(u32)tag64, (u32)(tag64 >> 32)};
  if (w == 0) {
    const u32 v = lane < 32 ? (lane * 8 + c) * 512 + (i >> 3) * 128 + (i & 7) * 8 : (i * 8 + (lane & 7)) * 512 + ((lane >> 3) & 3) * 128 + 64 + c * 8;
    st8c(k.ws, v, O_FLAG, tag);
  }
  SB();
#pragma unroll
  for (int n = NA1; n < 64; ++n) LDA(n);
  SB();
#define PERM(n) asm volatile("ds_write_b128 %1, %0 offset:%3\n\tds_read_b128 %0, %2 offset:%3" : "+a"(A[n]) : "v"(stw), "v"(ard), "i"(((n) % 6) * 1024) : "memory")
  const u32 ard = strd + ((q ^ gx) << 4);
#pragma unroll
  for (int n = 0; n < NA1; ++n) { PERM(n); if ((n & 3) == 3) asm volatile("s_waitcnt lgkmcnt(6)" ::: "memory"); }
  SB();
  STAMP(5);
  float sumw;
  {
    const v4u wv = ld16(mkrs(wr, 1024), lane * 16, 0);
    f2 e[4];
#pragma unroll
    for (int j = 0; j < 4; ++j) e[j] = unpk(wv[j]);
    const f2 sw = (e[0] + e[1]) + (e[2] + e[3]);
    sumw = bitsf(rfl(fbits(wavesum(sw[0] + sw[1]))));
    if (w == 0) { const u32 a = opq(L_WRF + ((lane * 32 - cg * 512) & 2047)); stl<f4>(a, 0, f4{e[0][0], e[0][1], e[1][0], e[1][1]}); stl<f4>(a, 16, f4{e[2][0], e[2][1], e[3][0], e[3][1]}); }
  }
  for (int sp = 0; sp < SPIN_MAX; ++sp) {
    const v2u f = __builtin_amdgcn_raw_buffer_load_b64(k.ws, (lane & 15) * 8, O_FLAG + me * 512 + w * 128, SC1);
    asm volatile("" ::: "memory");
    if (__all(f[0] == tag[0] && f[1] == tag[1])) break;
  }
  STAMP(6);
  v4u B[16];
  const u32 sb0 = O_AB0 + c * 524288 + w * 32768, sb1 = O_AB1 + c * 524288 + w * 32768;
#pragma unroll
  for (int n = 0; n < 16; ++n) { B[n] = ldb<0>(k, sb0, n); SB(); }
#pragma unroll
  for (int n = NA1; n < 64; ++n) { PERM(n); if ((n & 3) == 3) asm volatile("s_waitcnt lgkmcnt(6)" ::: "memory"); }
  bar();
  STAMP(7);

  v4u PQ[16][2];
  const u32 pqs = O_PQ0 + (i * 4 + w) * 32768;
  auto ldpq = [&](int s, int m) __attribute__((always_inline)) { return ld16c(k.ws, k.l16 + (s & 3) * 1024, pqs + m * 16384 + (((s >> 2) + cg) & 3) * 4096); };
  gemm2<0>(k, A, B, sb0, xres, [&](auto ic) __attribute__((always_inline)) {
    constexpr int n = ic;
    if constexpr (n >= 112 && n < 120) { constexpr int u = n - 112, s = (u & 3) * 4 + (u >> 2); PQ[s][0] = ldpq(s, 0); PQ[s][1] = ldpq(s, 1); }
  });
  STAMP(8);
  const bool vA = r5 < 28;
  const u32 jA = x8 * 28 + (vA ? r5 : 0), jB = x8 * 32 + r5;
  v4u GU[4]; u32 Dw[8];
  {
    const rsrc_t rg = mkrs(wg, 512 * 3584), ru = mkrs(wu, 512 * 3584);
    GU[0] = ld16(rg, tid * 7168, jA * 16); GU[1] = ld16(rg, tid * 7168 + 3584, jA * 16);
    GU[2] = ld16(ru, tid * 7168, jA * 16); GU[3] = ld16(ru, tid * 7168 + 3584, jA * 16);
  }
  SB();
  f2 sw = {0.f, 0.f};
  s1 = f2{0.f, 0.f}; s2 = f2{0.f, 0.f};
  const u32 wra = opq(L_WRF + sq * 32);
  f4 wq[2][2];
  auto ldwq = [&](int s, int p) __attribute__((always_inline)) { wq[p][0] = ldl<f4>(wra, s * 128); wq[p][1] = ldl<f4>(wra, s * 128 + 16); };
  auto take = [&](int s, int p) __attribute__((always_inline)) {
    const f4 w0 = wq[p][0], w1 = wq[p][1];
    const f2 wf[4] = {f2{w0[0], w0[1]}, f2{w0[2], w0[3]}, f2{w1[0], w1[1]}, f2{w1[2], w1[3]}};
#pragma unroll
    for (int j = 0; j < 4; ++j) { X[s][j] = unpk(PQ[s][0][j]) - unpk(PQ[s][1][j]); s1 += X[s][j]; s2 += X[s][j] * X[s][j]; sw += X[s][j] * wf[j]; }
    asm volatile("" : "+v"(X[s][0]), "+v"(X[s][1]), "+v"(X[s][2]), "+v"(X[s][3]), "+v"(s1), "+v"(s2), "+v"(sw));
  };
  auto ldq = [&](int r) __attribute__((always_inline)) {
#pragma unroll
    for (int s = r; s < 16; s += 4) { PQ[s][0] = ldpq(s, 0); PQ[s][1] = ldpq(s, 1); }
  };
  auto badq = [&](int r) __attribute__((always_inline)) {
    u32 mx = 0;
#pragma unroll
    for (int s = r; s < 16; s += 4) mx = umax(mx, umax(max4(PQ[s][0]), max4(PQ[s][1])));
    return __any(mx == SENT);
  };
  auto pollq = [&](int r) __attribute__((always_inline)) {
    for (int sp = 0; sp < SPIN_MAX; ++sp) { ldq(r); asm volatile("" ::: "memory"); if (!badq(r)) break; }
  };
  auto takeq = [&](int r) __attribute__((always_inline)) {
#pragma unroll
    for (int u = 0; u < 4; ++u) { if (u < 3) ldwq(4 * u + 4 + r, (u + 1) & 1); else if (r < 3) ldwq(r + 1, 0); SB(); take(4 * u + r, u & 1); SB(); }
  };
  ldq(2);
  ldwq(0, 0);
  SB();
  takeq(0); takeq(1);
  STAMP(9);
  if (badq(2)) pollq(2);
  ldq(3);
  SB();
  takeq(2);
  if (badq(3)) pollq(3);
  STAMP(10);
  {
    const rsrc_t rd = mkrs(wd, 1792 * 1024);
#pragma unroll
    for (int j = 0; j < 8; ++j) Dw[j] = ld4(rd, tid * 8192 + j * 1024, jB * 4);
  }
  SB();
  takeq(3);
  if (__builtin_expect(__any(s1[0] + s1[1] != s1[0] + s1[1]), 0)) {
    s1 = f2{0.f, 0.f}; s2 = f2{0.f, 0.f}; sw = f2{0.f, 0.f};
#pragma unroll
    for (int s = 0; s < 16; ++s) {
      PQ[s][0] = poll16(k.ws, k.l16 + (s & 3) * 1024, pqs + (((s >> 2) + cg) & 3) * 4096);
      PQ[s][1] = poll16(k.ws, k.l16 + (s & 3) * 1024, pqs + 16384 + (((s >> 2) + cg) & 3) * 4096);
      ldwq(s, 0); take(s, 0);
    }
  }
  mean = xsum32(xsum16(s1[0] + s1[1])) * (1.f / 512); var = fmaxf(xsum32(xsum16(s2[0] + s2[1])) * (1.f / 512) - mean * mean, 0.f);
  rstd = __builtin_amdgcn_rsqf(var + 1e-5f);
  if (me == 0 && tid == 0) st8c(k.ws, 0, O_EPOCH, v2u{(u32)(epoch + 1), (u32)((epoch + 1) >> 32)});
  if (!(c & 1) && w == (c >> 1)) {
    const float r = (xsum32(xsum16(sw[0] + sw[1])) - mean * sumw) * rstd;
    const u32 key = mkkey(r, 64 * i + 16 * w + t16), best = rowmax(key);
    const int src = __builtin_ctzll(__ballot(key == best));
    const v4u rec = {best, san(__builtin_amdgcn_readlane(fbits(r), src)), san(__builtin_amdgcn_readlane(fbits(mean), src)), san(__builtin_amdgcn_readlane(fbits(rstd), src))};
    if (lane < 8) st16c(k.ws, lane * 2048, O_KEY1 + (i * 4 + w) * 16, rec);
  }
  const float g1 = 1.f + rstd, s3 = g1 * __builtin_amdgcn_rsqf(var * g1 * g1 + 1e-5f);
#pragma unroll
  for (int s = 0; s < 4; ++s)
#pragma unroll
    for (int j = 0; j < 4; ++j) xres[s][j] = pk(X[s][j] * g1 - mean * rstd) & rmask;
  asm volatile("" : "+v"(xres[0]), "+v"(xres[1]), "+v"(xres[2]), "+v"(xres[3]));
  STAMP(11);
  u32 gate;
  const u32 gv = (lane >> 2) * 2048 + (lane & 3) * 512 + 496;
  gemm1<1>(k, X, s3, -mean * s3, [&](auto ic) __attribute__((always_inline)) {
    constexpr int n = ic;
    if constexpr (n == 40) gate = ld4c(k.ws, gv, sb1);
    if constexpr (n == 52) {
      if (__any(gate == SENT)) poll4(k.ws, gv, sb1);
#pragma unroll
      for (int u = 0; u < 16; ++u) { B[u] = ldb<1>(k, sb1, u); SB(); }
    }
  });
  STAMP(12);
  v4u K0, K1, HH, R0, R1, YY;
  u32 zp, zq, idx1 = 0; float r1 = 0.f, m1 = 0.f, rs1 = 0.f;
  v4u P5[2][2], DL[2][2][2], WV[2]; u32 cs = 0, ss = 0;
  const u32 p5s = O_PQ1 + me * 16384 + w * 2048;
  gemm2<1>(k, A, B, sb1, xres, [&](auto ic) __attribute__((always_inline)) {
    constexpr int n = ic;
    if constexpr (n == 0) { K0 = ld16c(k.ws, k.l16, O_KEY1 + k.rep * 2048); K1 = ld16c(k.ws, k.l16 + 1024, O_KEY1 + k.rep * 2048); }
    if constexpr (n == 10) {
      if (__any(umax(max4(K0), max4(K1)) == SENT)) { K0 = poll16(k.ws, k.l16, O_KEY1 + k.rep * 2048); K1 = poll16(k.ws, k.l16 + 1024, O_KEY1 + k.rep * 2048); }
      const v4u kk = K0[0] > K1[0] ? K0 : K1;
      const u32 best = wavemax(kk[0]);
      const int src = __builtin_ctzll(__ballot(kk[0] == best));
      idx1 = 2047u - (rfl(best) & 2047u);
      r1 = bitsf(__builtin_amdgcn_readlane(kk[1], src)); m1 = bitsf(__builtin_amdgcn_readlane(kk[2], src)); rs1 = bitsf(__builtin_amdgcn_readlane(kk[3], src));
      const u32 ch = (tid >> 2) & 3, v = (tid >> 4) * 1024 + (((ch & 1) << 1) | (ch >> 1)) * 256 + (tid & 3) * 4;
      zp = ld4c(k.ws, v, O_PQ0 + (idx1 >> 4) * 32768 + (idx1 & 15) * 16); zq = ld4c(k.ws, v + 16384, O_PQ0 + (idx1 >> 4) * 32768 + (idx1 & 15) * 16);
    }
    if constexpr (n == 22) {
      if (__any(umax(zp, zq) == SENT)) {
        const u32 ch = (tid >> 2) & 3, v = (tid >> 4) * 1024 + (((ch & 1) << 1) | (ch >> 1)) * 256 + (tid & 3) * 4;
        zp = poll4(k.ws, v, O_PQ0 + (idx1 >> 4) * 32768 + (idx1 & 15) * 16); zq = poll4(k.ws, v + 16384, O_PQ0 + (idx1 >> 4) * 32768 + (idx1 & 15) * 16);
      }
      const f2 z = unpk(pk((unpk(zp) - unpk(zq) - m1) * rs1));
      gemv1_part(k, z[0], z[1], GU);
    }
    if constexpr (n == 27) { if (w == 0) gemv1_fin(k, O_H1, jA, vA); }
    if constexpr (n == 48) HH = ld16c(k.ws, tid * 16, O_H1 + k.rep * 4096);
    if constexpr (n == 60) {
      if (__any(tid < 224 && max4(HH) == SENT)) HH = poll16(k.ws, tid * 16, O_H1 + k.rep * 4096, tid < 224);
      gemv2_part(k, HH, Dw);
    }
    if constexpr (n == 71) { if (w == 0) { const f2 y = gemv2_fin(r1); if (lane < 8) st4c(k.ws, lane * 2048, O_Y1 + jB * 4, san(pk(y))); } }
    if constexpr (n == 92) {
      const u32 ch = lane & 3, v = (lane >> 2) * 1024 + (((ch & 1) << 1) | (ch >> 1)) * 256;
      R0 = ld16c(k.ws, v, O_PQ0 + (idx1 >> 4) * 32768 + (idx1 & 15) * 16); R1 = ld16c(k.ws, v + 16384, O_PQ0 + (idx1 >> 4) * 32768 + (idx1 & 15) * 16);
      YY = ld16c(k.ws, k.l16, O_Y1 + k.rep * 2048);
    }
    if constexpr (n == 104) {
      if (__any(umax(max4(YY), umax(max4(R0), max4(R1))) == SENT)) {
        const u32 ch = lane & 3, v = (lane >> 2) * 1024 + (((ch & 1) << 1) | (ch >> 1)) * 256;
        R0 = poll16(k.ws, v, O_PQ0 + (idx1 >> 4) * 32768 + (idx1 & 15) * 16); R1 = poll16(k.ws, v + 16384, O_PQ0 + (idx1 >> 4) * 32768 + (idx1 & 15) * 16);
        YY = poll16(k.ws, k.l16, O_Y1 + k.rep * 2048);
      }
      const float ga = 1.f + rs1, va = fmaxf(1.f / (rs1 * rs1) - 1e-5f, 0.f), s3o = ga * __builtin_amdgcn_rsqf(va * ga * ga + 1e-5f);
      f2 xn[4], zo[4], a1 = {0.f, 0.f}, a2 = {0.f, 0.f};
      const u32 dxa = opq(L_DX + lane * 32);
#pragma unroll
      for (int j = 0; j < 4; ++j) {
        const f2 x1 = unpk(R0[j]) - unpk(R1[j]);
        const f2 z2 = unpk(pk((x1 - m1) * rs1));
        xn[j] = unpk(pk(x1 + unpk(pk(z2 + unpk(YY[j])))));
        zo[j] = unpk(pk(x1 * s3o - m1 * s3o));
        const f2 dx = xn[j] - unpk(pk(x1 * ga - m1 * rs1));
        if (w == 0) stl<f2>(dxa, j * 8, dx);
        a1 += xn[j]; a2 += xn[j] * xn[j];
      }
      const float mn = wavesum(a1[0] + a1[1]) * (1.f / 512), vn = fmaxf(wavesum(a2[0] + a2[1]) * (1.f / 512) - mn * mn, 0.f), rn = __builtin_amdgcn_rsqf(vn + 1e-5f);
      const u32 e = 4 * i + w;
      const v4u wv = ldl<v4u>(L_W + e * 1024 + ((((lane >> 4) - cg) & 3) * 16 + ((lane ^ e) & 15)) * 16);
      f2 d = {0.f, 0.f};
#pragma unroll
      for (int j = 0; j < 4; ++j) d += (unpk(pk((xn[j] - mn) * rn)) - zo[j]) * unpk(wv[j]);
      const float dl = wavesum(d[0] + d[1]);
      if (lane < 8) st4c(k.ws, lane * 4096, O_DEL + (128 * c + e) * 4, san(fbits(dl)));
    }
    if constexpr (n == 114) {
      P5[0][0] = ld16c(k.ws, k.l16, p5s); P5[0][1] = ld16c(k.ws, k.l16, p5s + 8192);
      const rsrc_t rr = mkrs(wr, 1024);
      WV[0] = ld16(rr, (lane >> 3) * 16, w * 256); WV[1] = ld16(rr, (lane >> 3) * 16 + 128, w * 256);
      const u32 v = (64 * i + 8 * c + (lane & 7)) * 4096;
      cs = __builtin_amdgcn_raw_buffer_load_b16(mkrs(Cs, 2048 * 4096), v, idx1 * 2, 0); ss = __builtin_amdgcn_raw_buffer_load_b16(mkrs(Ss, 2048 * 4096), v, idx1 * 2, 0);
    }
  });
  STAMP(13);
  const u32 dv = opq((lane >> 3) * 32), dso = O_DEL + k.rep * 4096 + w * 512;
  for (int sp = 0; sp < SPIN_MAX; ++sp) {
    u32 mx = 0;
#pragma unroll
    for (int n = 0; n < 2; ++n)
#pragma unroll
      for (int m = 0; m < 2; ++m)
#pragma unroll
        for (int h = 0; h < 2; ++h) { DL[n][m][h] = ld16c(k.ws, dv + n * 256 + h * 16 + m * 2048, dso); }
    asm volatile("" ::: "memory");
#pragma unroll
    for (int n = 0; n < 2; ++n)
#pragma unroll
      for (int m = 0; m < 2; ++m) mx = umax(mx, umax(max4(DL[n][m][0]), max4(DL[n][m][1])));
    if (!__any(mx == SENT)) break;
  }
  if (__any(umax(max4(P5[0][0]), max4(P5[0][1])) == SENT)) { P5[0][0] = poll16(k.ws, k.l16, p5s); P5[0][1] = poll16(k.ws, k.l16, p5s + 8192); }
  STAMP(14);
  for (int sp = 0; sp < SPIN_MAX; ++sp) {
    P5[1][0] = ld16c(k.ws, k.l16 + 1024, p5s); P5[1][1] = ld16c(k.ws, k.l16 + 1024, p5s + 8192);
    asm volatile("" ::: "memory");
    if (!__any(umax(max4(P5[1][0]), max4(P5[1][1])) == SENT)) break;
  }
  STAMP(15);
  const u32 row = 64 * i + 8 * c + (lane & 7);
  f2 Y[2][4];
  {
    const float fc = blo(cs), fs = blo(ss);
    const u32 dxr = opq(L_DX + (16 * w + (lane >> 3)) * 32);
    s1 = f2{0.f, 0.f}; s2 = f2{0.f, 0.f}; sw = f2{0.f, 0.f};
#pragma unroll
    for (int n = 0; n < 2; ++n)
#pragma unroll
      for (int j = 0; j < 4; ++j) {
        const f2 da = {bitsf(DL[n][0][j >> 1][2 * (j & 1)]), bitsf(DL[n][0][j >> 1][2 * (j & 1) + 1])}, db = {bitsf(DL[n][1][j >> 1][2 * (j & 1)]), bitsf(DL[n][1][j >> 1][2 * (j & 1) + 1])};
        f2 v = unpk(P5[n][0][j]) - unpk(P5[n][1][j]) + (da * fc - db * fs);
        const f2 dx = ldl<f2>(dxr, n * 256 + j * 8);
        if (row == idx1) v += dx;
        v = unpk(pk(v));
        Y[n][j] = v; s1 += v; s2 += v * v; sw += v * unpk(WV[n][j]);
      }
    float a = s1[0] + s1[1], bb = s2[0] + s2[1], cc = sw[0] + sw[1];
    a += dppf<0x128>(a); bb += dppf<0x128>(bb); cc += dppf<0x128>(cc);
    a = xsum32(xsum16(a)); bb = xsum32(xsum16(bb)); cc = xsum32(xsum16(cc));
    if (lane < 8) stl<f4>(opq(L_ST + (w * 8 + lane) * 16), 0, f4{a, bb, cc, 0.f});
  }
  bar();
  const u32 sta = opq(L_ST + (lane & 7) * 16);
  const f4 st4 = (ldl<f4>(sta) + ldl<f4>(sta, 128)) + (ldl<f4>(sta, 256) + ldl<f4>(sta, 384));
  mean = st4[0] * (1.f / 512); var = fmaxf(st4[1] * (1.f / 512) - mean * mean, 0.f); rstd = __builtin_amdgcn_rsqf(var + 1e-5f);
  const float r4 = (st4[2] - mean * sumw) * rstd;
  const u32 key = mkkey(r4, row);
  u32 best = umax(key, dppu<0x141>(key)); best = umax(best, dppu<0xB1>(best)); best = umax(best, dppu<0x4E>(best));
  const bool mine = key == best;
  if (w == 0) {
    const int src = __builtin_ctzll(__ballot(mine));
    if (lane < 8) st8c(k.ws, lane * 2048, O_KEY2 + me * 8, v2u{best, san(__builtin_amdgcn_readlane(fbits(r4), src))});
  }
  const rsrc_t ro = mkrs(out, 2048 * 1024);
  v4u O[2];
#pragma unroll
  for (int n = 0; n < 2; ++n) {
    v4u z, xx;
#pragma unroll
    for (int j = 0; j < 4; ++j) { z[j] = san(pk((Y[n][j] - mean) * rstd)); xx[j] = san(pk(Y[n][j])); O[n][j] = pk(Y[n][j] + unpk(z[j])); }
    const u32 cb = (16 * w + 8 * n + (lane >> 3)) * 16;
    if (mine) { st16c(k.ws, cb, O_CAND + me * 2048, z); st16c(k.ws, cb + 1024, O_CAND + me * 2048, xx); }
    else __builtin_amdgcn_raw_buffer_store_b128(O[n], ro, row * 1024 + cb, 0, 0);
  }
  STAMP(16);
  if (w == 0) {
    v4u a, bb;
    for (int sp = 0; sp < SPIN_MAX; ++sp) {
      a = ld16c(k.ws, k.l16, O_KEY2 + k.rep * 2048); bb = ld16c(k.ws, k.l16 + 1024, O_KEY2 + k.rep * 2048);
      asm volatile("" ::: "memory");
      if (!__any(umax(max4(a), max4(bb)) == SENT)) break;
    }
    u32 kk = a[0], rr = a[1];
    if (a[2] > kk) { kk = a[2]; rr = a[3]; }
    if (bb[0] > kk) { kk = bb[0]; rr = bb[1]; }
    if (bb[2] > kk) { kk = bb[2]; rr = bb[3]; }
    const u32 gb = wavemax(kk);
    if (kk == gb) stl<v2u>(opq(L_BC), 0, v2u{gb, rr});
  }
  bar();
  STAMP(17);
  const v2u gb = ldl<v2u>(opq(L_BC));
  const u32 idx2 = 2047u - (rfl(gb[0]) & 2047u);
  const u32 cso = O_CAND + (idx2 >> 3) * 2048;
  u32 zz = ld4c(k.ws, tid * 4, cso), fz = ld4c(k.ws, 0, cso + jB * 4), fx = ld4c(k.ws, 1024, cso + jB * 4);
  if (mine && row != idx2) {
#pragma unroll
    for (int n = 0; n < 2; ++n) __builtin_amdgcn_raw_buffer_store_b128(O[n], ro, row * 1024 + (16 * w + 8 * n + (lane >> 3)) * 16, 0, 0);
  }
  if (__any(zz == SENT)) zz = poll4(k.ws, tid * 4, cso);
  gemv1_part(k, blo(zz), bhi(zz), GU);
  bar();
  if (w == 0) gemv1_fin(k, O_H2, jA, vA);
  STAMP(18);
  HH = poll16(k.ws, tid * 16, O_H2 + k.rep * 4096, tid < 224);
  STAMP(19);
  gemv2_part(k, HH, Dw);
  bar();
  if (w == 0) {
    if (__any(umax(fz, fx) == SENT)) { fz = poll4(k.ws, 0, cso + jB * 4); fx = poll4(k.ws, 1024, cso + jB * 4); }
    const f2 y = gemv2_fin(bitsf(gb[1]));
    const u32 o = pk(unpk(fx) + unpk(pk(unpk(fz) + y)));
    if (lane == 0) __builtin_amdgcn_raw_buffer_store_b32(o, ro, idx2 * 1024 + jB * 4, 0, 0);
  }
  STAMP(20);
  asm volatile("fm_code_end:");
}

extern "C" void kernel_launch(void* const* d_in, const int* in_sizes, int n_in, void* d_out, int out_size, void* d_ws, size_t ws_size, hipStream_t stream) {
  (void)hipFuncSetAttribute((const void*)fnet_mod_kernel, hipFuncAttributeMaxDynamicSharedMemorySize, LDS_BYTES);
  fnet_mod_kernel<<<256, 256, LDS_BYTES, stream>>>((const u16*)d_in[0], (const u16*)d_in[1], (const u16*)d_in[2], (const u16*)d_in[3], (const u16*)d_in[4], (const u16*)d_in[6],
                                                   (const u16*)d_in[7], (const u16*)d_in[8], (const u16*)d_in[9], (u16*)d_out, (unsigned char*)d_ws);
}
```

```cpp
#define PFA 0
#define NT_F 0
#define WS_END 1
#include <hip/hip_runtime.h>
#include <hip/hip_bf16.h>
#include <stdint.h>
#include <utility>

typedef unsigned u32;
typedef unsigned long long u64;
typedef unsigned short u16;
typedef u32 v4u __attribute__((ext_vector_type(4)));
typedef u32 v2u __attribute__((ext_vector_type(2)));
typedef float f2 __attribute__((ext_vector_type(2)));
typedef float f4 __attribute__((ext_vector_type(4)));
typedef __bf16 bf2 __attribute__((ext_vector_type(2)));
typedef __amdgpu_buffer_rsrc_t rsrc_t;
#define DI __device__ __forceinline__
#define LAS __attribute__((address_space(3)))
#define SB() __builtin_amdgcn_sched_barrier(0)

#define NT_A ""
#define NT_X ""
constexpr u32 SENT = 0xFFFFFFFFu;
constexpr int SC1 = 16;
constexpr int SPIN_MAX = 200000;
constexpr u32 O_EPOCH = 0, O_FLAG = 4096, O_SMALL = 0x40000;
constexpr u32 O_KEY1 = O_SMALL, O_H1 = O_SMALL + 16384, O_Y1 = O_SMALL + 49152, O_DEL = O_SMALL + 65536, O_KEY2 = O_SMALL + 98304, O_H2 = O_SMALL + 114688;
constexpr u32 SMALL_KB = 144;
constexpr u32 O_CAND = 0x80000, O_AB0 = 1u << 20, O_AB1 = 5u << 20, O_PQ0 = 9u << 20, O_PQ1 = 13u << 20, WS_BYTES = 17u << 20;
constexpr u32 O_DBG = 32u << 20;
constexpr u32 L_W = 0, L_RED = 131072, L_WRF = 155648, L_DX = 157696, L_P1 = 159744, L_P2 = 160000, L_ST = 160064, L_BC = 160576, LDS_BYTES = 163840;

DI rsrc_t mkrs(const void* p, u32 bytes) { return __builtin_amdgcn_make_buffer_rsrc((void*)p, 0, bytes, 0x00020000); }
DI v4u ld16(rsrc_t r, u32 v, u32 s) { return __builtin_amdgcn_raw_buffer_load_b128(r, v, s, 0); }
DI v4u ld16n(rsrc_t r, u32 v, u32 s) { return __builtin_amdgcn_raw_buffer_load_b128(r, v, s, NT_F); }
DI u32 ld4n(rsrc_t r, u32 v, u32 s) { return __builtin_amdgcn_raw_buffer_load_b32(r, v, s, NT_F); }
DI v4u ld16c(rsrc_t r, u32 v, u32 s) { return __builtin_amdgcn_raw_buffer_load_b128(r, v, s, SC1); }
DI u32 ld4(rsrc_t r, u32 v, u32 s) { return __builtin_amdgcn_raw_buffer_load_b32(r, v, s, 0); }
DI u32 ld4c(rsrc_t r, u32 v, u32 s) { return __builtin_amdgcn_raw_buffer_load_b32(r, v, s, SC1); }
DI void st16c(rsrc_t r, u32 v, u32 s, v4u d) { __builtin_amdgcn_raw_buffer_store_b128(d, r, v, s, SC1); }
DI void st8c(rsrc_t r, u32 v, u32 s, v2u d) { __builtin_amdgcn_raw_buffer_store_b64(d, r, v, s, SC1); }
DI void st4c(rsrc_t r, u32 v, u32 s, u32 d) { __builtin_amdgcn_raw_buffer_store_b32(d, r, v, s, SC1); }
DI float blo(u32 d) { return __builtin_bit_cast(float, d << 16); }
DI float bhi(u32 d) { return __builtin_bit_cast(float, d & 0xffff0000u); }
DI f2 unpk(u32 d) { return f2{blo(d), bhi(d)}; }
DI u32 pk(f2 v) { return __builtin_bit_cast(u32, __builtin_convertvector(v, bf2)); }
DI float rbf(float v) { return blo(pk(f2{v, 0.f})); }
DI u32 san(u32 d) { return d < SENT ? d : SENT - 1u; }
DI u32 fbits(float f) { return __builtin_bit_cast(u32, f); }
DI float bitsf(u32 u) { return __builtin_bit_cast(float, u); }
DI u32 max4(v4u a) { u32 m = a[0] > a[1] ? a[0] : a[1]; u32 n = a[2] > a[3] ? a[2] : a[3]; return m > n ? m : n; }
DI u32 umax(u32 a, u32 b) { return a > b ? a : b; }
template <int C> DI float dppf(float x) { return bitsf(__builtin_amdgcn_mov_dpp((int)fbits(x), C, 0xf, 0xf, true)); }
template <int C> DI u32 dppu(u32 x) { return (u32)__builtin_amdgcn_mov_dpp((int)x, C, 0xf, 0xf, true); }
DI float xsum16(float x) { auto s = __builtin_amdgcn_permlane16_swap(fbits(x), fbits(x), false, false); return bitsf(s[0]) + bitsf(s[1]); }
DI float xsum32(float x) { auto s = __builtin_amdgcn_permlane32_swap(fbits(x), fbits(x), false, false); return bitsf(s[0]) + bitsf(s[1]); }
DI float rowsum(float x) { x += dppf<0x128>(x); x += dppf<0x124>(x); x += dppf<0x122>(x); x += dppf<0x121>(x); return x; }
DI float wavesum(float x) { return xsum32(xsum16(rowsum(x))); }
DI u32 rowmax(u32 x) { x = umax(x, dppu<0x128>(x)); x = umax(x, dppu<0x124>(x)); x = umax(x, dppu<0x122>(x)); x = umax(x, dppu<0x121>(x)); return x; }
DI u32 wavemax(u32 x) {
  x = rowmax(x);
  auto s = __builtin_amdgcn_permlane16_swap(x, x, false, false); x = umax(s[0], s[1]);
  auto t = __builtin_amdgcn_permlane32_swap(x, x, false, false); return umax(t[0], t[1]);
}
DI u32 mkkey(float r, u32 row) { u32 u = fbits(r); u = (u & 0x80000000u) ? ~u : (u | 0x80000000u); return san((u & ~2047u) | (2047u - row)); }
DI u32 rfl(u32 x) { return __builtin_amdgcn_readfirstlane(x); }
DI u32 opq(u32 x) { asm volatile("" : "+v"(x)); return x; }
DI void bar() { asm volatile("s_waitcnt lgkmcnt(0)\n\ts_barrier" ::: "memory"); }
template <typename T> DI T ldl(u32 a, u32 o = 0) { return *(const LAS T*)((const LAS char*)(size_t)a + o); }
template <typename T> DI void stl(u32 a, u32 o, T v) { *(LAS T*)((LAS char*)(size_t)a + o) = v; }
#define MFMA0(acc, a, b, CA, CB) asm volatile("v_mfma_f32_16x16x32_bf16 %0, %1, %2, 0" : "=&v"(acc) : CA(a), CB(b))
#define MFMA(acc, a, b, CA, CB) asm volatile("v_mfma_f32_16x16x32_bf16 %0, %1, %2, %0" : "+v"(acc) : CA(a), CB(b))
#define VM(n) asm volatile("s_waitcnt vmcnt(%0)" :: "i"(n) : "memory")

template <int... I, typename F> DI void sfor_(std::integer_sequence<int, I...>, F&& f) { (f(std::integral_constant<int, I>{}), ...); }
template <int N, typename F> DI void sfor(F&& f) { sfor_(std::make_integer_sequence<int, N>{}, f); }

#define STAMP(n)
#define STAMPX(set, n)

#define NA1 44

constexpr int cntA(int n) {
  const int v = n < 0 ? 0 : n < 26 ? n / 2 + 1 : 13 + (n - 25);
  return v < NA1 ? v : NA1;
}

struct Ctx {
  rsrc_t ws;
  u32 lane, l16, w, tid, i, c, cg, im, rep;
};

template <int L, typename HOOK>
DI void gemm1(const Ctx& k, f2 (&X)[16][4], float sc, float of, HOOK&& hook) {
  v4u Z[16], Wf[8];
  f4 acc[2][2];
  const u32 ln = opq(k.lane), t16 = ln & 15, q = ln >> 4, sq = ((q & 1) << 1) | (q >> 1);
  u32 wb[8];
#pragma unroll
  for (int h = 0; h < 8; ++h) wb[h] = opq(L_W + (h >> 2) * 65536 + t16 * 1024 + (((4 * (h & 3) + sq) ^ t16) << 4));
  const u32 abv = (ln >> 5) * 256 + (q & 1) * 1024 + t16 * 16;
  const u32 abs0 = (L ? O_AB1 : O_AB0) + k.c * 524288 + (2 * k.i + (k.w >> 1)) * 2048 + (k.w & 1) * 512;
  auto mkz = [&](int s) __attribute__((always_inline)) {
#pragma unroll
    for (int j = 0; j < 4; ++j) Z[s][j] = pk(X[s][j] * sc + of);
    asm volatile("" : "+v"(Z[s]));
  };
  auto ldw = [&](int n) __attribute__((always_inline)) {
    const int qd = n >> 4, s = n & 15;
#pragma unroll
    for (int j = 0; j < 2; ++j) { const int ct = 2 * qd + j; Wf[(2 * n + j) & 7] = ldl<v4u>(wb[(ct >> 2) * 4 + (s & 3)], (ct & 3) * 16384 + (s >> 2) * 256); }
  };
  auto fin = [&](int qd) __attribute__((always_inline)) {
    f4 a = acc[qd & 1][0], b = acc[qd & 1][1];
    u32 a0 = san(pk(f2{a[0], a[1]})), a1 = san(pk(f2{a[2], a[3]})), b0 = san(pk(f2{b[0], b[1]})), b1 = san(pk(f2{b[2], b[3]}));
    auto s0 = __builtin_amdgcn_permlane16_swap(a0, b0, false, false);
    auto s1 = __builtin_amdgcn_permlane16_swap(a1, b1, false, false);
    st16c(k.ws, abv, abs0 + qd * 131072, v4u{s0[0], s1[0], s0[1], s1[1]});
  };
#pragma unroll
  for (int n = 0; n < 4; ++n) ldw(n);
  mkz(0); mkz(1);
  asm volatile("s_nop 1" : "+v"(Z[0]), "+v"(Z[1]));
  SB();
  sfor<64>([&](auto ic) __attribute__((always_inline)) {
    constexpr int n = ic, qd = n >> 4, s = n & 15;
    if constexpr (s == 0) { MFMA0(acc[qd & 1][0], Z[s], Wf[(2 * n) & 7], "v", "v"); MFMA0(acc[qd & 1][1], Z[s], Wf[(2 * n + 1) & 7], "v", "v"); }
    else { MFMA(acc[qd & 1][0], Z[s], Wf[(2 * n) & 7], "v", "v"); MFMA(acc[qd & 1][1], Z[s], Wf[(2 * n + 1) & 7], "v", "v"); }
    SB();
    if constexpr (n + 4 < 64) ldw(n + 4);
    if constexpr (n + 2 < 16) mkz(n + 2);
    if constexpr (qd > 0 && s == 4) fin(qd - 1);
    hook(ic);
    SB();
  });
  asm volatile("s_nop 7" : "+v"(acc[1][0]), "+v"(acc[1][1]));
  fin(3);
}

template <int L> DI v4u ldb(const Ctx& k, u32 sb, int n) { return ld16c(k.ws, k.l16 + (n & 3) * 1024, sb + (n >> 5) * 131072 + ((n & 31) >> 2) * 4096); }

template <int L> DI void slowpass(const Ctx& k, u32 sb, int qd, const v4u (&A)[64], f4 (&acc)[4][2]) {
#pragma unroll
  for (int n = 0; n < 32; ++n) {
    v4u b;
    for (int sp = 0; sp < SPIN_MAX; ++sp) {
      b = ldb<L>(k, sb, qd * 32 + n);
      asm volatile("" ::: "memory");
      if (!__any(max4(b) == SENT)) break;
    }
#pragma unroll
    for (int T = 0; T < 4; ++T) { if (n < 2) MFMA0(acc[T][n & 1], b, A[T * 16 + (n >> 1)], "v", "a"); else MFMA(acc[T][n & 1], b, A[T * 16 + (n >> 1)], "v", "a"); }
    SB();
  }
  asm volatile("s_nop 7" : "+v"(acc[0][0]), "+v"(acc[0][1]), "+v"(acc[1][0]), "+v"(acc[1][1]), "+v"(acc[2][0]), "+v"(acc[2][1]), "+v"(acc[3][0]), "+v"(acc[3][1]));
}

template <int L, typename HOOK>
DI void gemm2(const Ctx& k, const v4u (&A)[64], v4u (&B)[16], u32 sb, const v4u (&xres)[4], HOOK&& hook) {
  f4 acc[2][4][2], rd[3][2];
  const u32 ln = opq(k.lane), q = ln >> 4, t = ln & 15, sq = ((q & 1) << 1) | (q >> 1);
  const u32 pv = L ? (t >> 3) * 16384 + sq * 128 + (t & 7) * 16 : k.l16;
  const u32 ps = L ? O_PQ1 + (k.i * 8 + 2 * k.w) * 16384 + k.im * 8192 + k.cg * 2048 : O_PQ0 + ((k.i * 4 + k.w) * 2 + k.im) * 16384 + k.cg * 4096;
  auto check = [&](int qd) __attribute__((always_inline)) {
    if (L == 1) {
      f4 s4 = acc[qd & 1][0][0] + acc[qd & 1][0][1];
      float s = (s4[0] + s4[1]) + (s4[2] + s4[3]);
      if (__builtin_expect(__any(s != s), 0)) slowpass<L>(k, sb, qd, A, acc[qd & 1]);
    }
  };
  auto wr = [&](int qd, int T) __attribute__((always_inline)) {
    const u32 a = opq(k.l16 + (L_RED + ((T + k.w) & 3) * 6144 + (T - 1) * 2048));
    stl<f4>(a, 0, acc[qd & 1][T][0]); stl<f4>(a, 1024, acc[qd & 1][T][1]);
  };
  auto rdl = [&](int u) __attribute__((always_inline)) { const u32 a = opq(k.l16 + (L_RED + k.w * 6144 + u * 2048)); rd[u][0] = ldl<f4>(a); rd[u][1] = ldl<f4>(a, 1024); };
  auto fin = [&](int qd) __attribute__((always_inline)) {
    f4 a = (acc[qd & 1][0][0] + rd[0][0]) + (rd[1][0] + rd[2][0]), b = (acc[qd & 1][0][1] + rd[0][1]) + (rd[1][1] + rd[2][1]);
    float v[8];
#pragma unroll
    for (int j = 0; j < 4; ++j) { auto s = __builtin_amdgcn_permlane16_swap(fbits(a[j]), fbits(b[j]), false, false); v[j] = bitsf(s[0]); v[4 + j] = bitsf(s[1]); }
    v4u o;
#pragma unroll
    for (int j = 0; j < 4; ++j) o[j] = san(pk(f2{v[2 * j], v[2 * j + 1]} + unpk(xres[qd][j])));
    st16c(k.ws, pv, ps + qd * (L ? 512 : 1024), o);
  };
  sfor<128>([&](auto ic) __attribute__((always_inline)) {
    constexpr int n = ic, qd = n >> 5, m = n & 31;
#pragma unroll
    for (int T = 0; T < 4; ++T) {
      if constexpr (m < 2) MFMA0(acc[qd & 1][T][m & 1], B[n & 15], A[T * 16 + (m >> 1)], "v", "a");
      else MFMA(acc[qd & 1][T][m & 1], B[n & 15], A[T * 16 + (m >> 1)], "v", "a");
    }
    SB();
    if constexpr (n + 16 < 128) B[n & 15] = ldb<L>(k, sb, n + 16);
    if constexpr (qd > 0) {
      if constexpr (m == 1) check(qd - 1);
      if constexpr (m >= 2 && m <= 4) wr(qd - 1, m - 1);
      if constexpr (m == 6) bar();
      if constexpr (m >= 7 && m <= 9) rdl(m - 7);
      if constexpr (m == 12) fin(qd - 1);
    }
    if constexpr (m == 26) bar();
    hook(ic);
    SB();
  });
  asm volatile("s_nop 7" : "+v"(acc[1][0][0]), "+v"(acc[1][0][1]), "+v"(acc[1][1][0]), "+v"(acc[1][1][1]), "+v"(acc[1][2][0]), "+v"(acc[1][2][1]), "+v"(acc[1][3][0]), "+v"(acc[1][3][1]));
  check(3);
  wr(3, 1); wr(3, 2); wr(3, 3);
  bar();
  rdl(0); rdl(1); rdl(2);
  fin(3);
}

DI v4u poll16(rsrc_t r, u32 v, u32 s, bool act = true) {
  v4u b;
  for (int sp = 0; sp < SPIN_MAX; ++sp) { b = ld16c(r, v, s); asm volatile("" ::: "memory"); if (!__any(act && max4(b) == SENT)) break; }
  return b;
}
DI u32 poll4(rsrc_t r, u32 v, u32 s) {
  u32 b;
  for (int sp = 0; sp < SPIN_MAX; ++sp) { b = ld4c(r, v, s); asm volatile("" ::: "memory"); if (!__any(b == SENT)) break; }
  return b;
}

DI void gemv1_part(const Ctx& k, float z0, float z1, const v4u (&GU)[4]) {
  float V[16];
#pragma unroll
  for (int h = 0; h < 2; ++h)
#pragma unroll
    for (int j = 0; j < 4; ++j) { f2 r = unpk(GU[2 * h][j]) * z0 + unpk(GU[2 * h + 1][j]) * z1; V[8 * h + 2 * j] = r[0]; V[8 * h + 2 * j + 1] = r[1]; }
  float S[8], T[4];
#pragma unroll
  for (int j = 0; j < 8; ++j) { auto s = __builtin_amdgcn_permlane32_swap(fbits(V[j]), fbits(V[j + 8]), false, false); S[j] = bitsf(s[0]) + bitsf(s[1]); }
#pragma unroll
  for (int j = 0; j < 4; ++j) { auto s = __builtin_amdgcn_permlane16_swap(fbits(S[j]), fbits(S[j + 4]), false, false); T[j] = rowsum(bitsf(s[0]) + bitsf(s[1])); }
  if ((k.lane & 15) == 0) stl<f4>(opq(L_P1 + k.w * 64 + (k.lane >> 4) * 16), 0, f4{T[0], T[1], T[2], T[3]});
}
DI void gemv1_fin(const Ctx& k, u32 oh, u32 jA, bool valid) {
  const u32 p = k.lane & 3, pa = opq(L_P1 + p * 8);
  f2 g = {0.f, 0.f}, u = {0.f, 0.f};
#pragma unroll
  for (int w = 0; w < 4; ++w) { g += ldl<f2>(pa, w * 64); u += ldl<f2>(pa, w * 64 + 32); }
  f2 h;
#pragma unroll
  for (int j = 0; j < 2; ++j) { const float gg = rbf(g[j]); h[j] = rbf(gg / (1.f + __expf(-gg))) * rbf(u[j]); }
  if (valid && k.lane < 32) st4c(k.ws, (k.lane >> 2) * 4096 + p * 4, oh + jA * 16, san(pk(h)));
}
DI void gemv2_part(const Ctx& k, v4u h, const u32 (&Dw)[8]) {
  f2 y = {0.f, 0.f};
#pragma unroll
  for (int j = 0; j < 4; ++j) { y += unpk(Dw[2 * j]) * blo(h[j]); y += unpk(Dw[2 * j + 1]) * bhi(h[j]); }
  if (k.tid >= 224) y = f2{0.f, 0.f};
  auto s = __builtin_amdgcn_permlane32_swap(fbits(y[0]), fbits(y[1]), false, false);
  const float v = rowsum(xsum16(bitsf(s[0]) + bitsf(s[1])));
  if ((k.lane & 31) == 0) stl<float>(opq(L_P2 + k.w * 8 + (k.lane >> 5) * 4), 0, v);
}
DI f2 gemv2_fin(float r) {
  const u32 pa = opq(L_P2);
  f2 y = (ldl<f2>(pa) + ldl<f2>(pa, 8)) + (ldl<f2>(pa, 16) + ldl<f2>(pa, 24));
  const float sg = rbf(1.f / (1.f + __expf(-rbf(r))));
  return f2{rbf(rbf(y[0]) * sg), rbf(rbf(y[1]) * sg)};
}

__global__ __launch_bounds__(256, 1) void fnet_mod_kernel(const u16* x, const u16* Cs, const u16* Ss, const u16* Cd, const u16* Sd, const u16* wr,
                                                          const u16* wg, const u16* wu, const u16* wd, u16* out, unsigned char* wsp) {
  extern __shared__ __attribute__((aligned(16))) unsigned char smem[];
  STAMP(0);
  Ctx k;
  k.tid = threadIdx.x; k.lane = k.tid & 63; k.w = rfl(k.tid >> 6);
  const u32 b = blockIdx.x, x8 = b & 7, r5 = b >> 3;
  k.c = (x8 & 1) * 4 + (r5 & 3); k.i = (x8 >> 1) * 8 + (r5 >> 2); k.cg = k.c & 3; k.im = k.c >> 2; k.rep = x8;
  k.ws = mkrs(wsp, WS_BYTES);
  k.l16 = opq(k.lane * 16);
  const u32 lane = k.lane, w = k.w, tid = k.tid, i = k.i, c = k.c, cg = k.cg, im = k.im, me = i * 8 + c;
  const u32 q = lane >> 4, t16 = lane & 15, sq = ((q & 1) << 1) | (q >> 1);
  asm volatile("" :: "v"((u32)(size_t)smem));

  u32 pfd, pfe; v2u ep;
  {
    u64 pc; u32 len;
    asm volatile("s_getpc_b64 %0\nfm_code_begin:\n\ts_mov_b32 %1, fm_code_end-fm_code_begin" : "=s"(pc), "=s"(len));
    u32 o = tid * 128, o2 = o + 32768; o = o < len ? o : 0; o2 = o2 < len ? o2 : 0;
    asm volatile("global_load_dword %0, %1, %2" : "=v"(pfd) : "v"(o), "s"(pc) : "memory");
    asm volatile("global_load_dword %0, %1, %2" : "=v"(pfe) : "v"(o2), "s"(pc) : "memory");
    asm volatile("buffer_load_dwordx2 %0, %1, %2, 0 offen sc1" : "=v"(ep) : "v"(0u), "s"(k.ws) : "memory");
  }
  STAMPX(2, 1);
  v4u XR[16];
  const rsrc_t rx = mkrs(x, 2048 * 1024);
  {
    const u32 v = (64 * i + 16 * w + (lane >> 2)) * 1024 + (lane & 3) * 16;
#pragma unroll
    for (int s = 0; s < 16; ++s)
      asm volatile("buffer_load_dwordx4 %0, %1, %2, %3 offen offset:%4" NT_X : "=v"(XR[s]) : "v"(v), "s"(rx), "s"((((s >> 2) + cg) & 3) * 256), "i"((s & 3) * 64) : "memory");
  }
  STAMPX(2, 2);
  const rsrc_t rw = mkrs(im ? Sd : Cd, 512 * 1024);
  const u32 wg0 = (((lane >> 4) + cg) & 3) * 256;
  auto ldW = [&](int n) __attribute__((always_inline)) {
    const u32 e = (n >> 3) * 32 + w * 8 + (n & 7);
    const u32 v = wg0 + (((lane ^ e) & 15) << 4);
    asm volatile("s_mov_b32 m0, %0\n\ts_nop 0\n\tbuffer_load_dwordx4 %1, %2, %3 offen" NT_X " lds" :: "s"(L_W + e * 1024), "v"(v), "s"(rw), "s"((128 * cg + e) * 1024) : "memory");
  };
#pragma unroll
  for (int n = 0; n < 16; ++n) ldW(n);
  STAMPX(2, 3);
  v4u A[64];
  const rsrc_t rm = mkrs(im ? Ss : Cs, 2048 * 4096);
  const u32 av = (lane >> 2) * 4096 + (lane & 3) * 16;
  u32 as[4];
#pragma unroll
  for (int T = 0; T < 4; ++T) as[T] = (64 * i + 16 * ((T + w) & 3)) * 4096 + w * 1024;
  u32 pa0 = 0, pa1 = 0;
  if (PFA) {
    asm volatile("buffer_load_dword %0, %1, %2, %3 offen nt" : "=v"(pa0) : "v"(tid * 128), "s"(rm), "s"((64 * i + 16 * cg) * 4096) : "memory");
    asm volatile("buffer_load_dword %0, %1, %2, %3 offen nt" : "=v"(pa1) : "v"(tid * 128), "s"(rm), "s"((64 * i + 16 * cg + 8) * 4096) : "memory");
  }
#define LDA(n) asm volatile("buffer_load_dwordx4 %0, %1, %2, %3 offen offset:%4" NT_A : "=a"(A[n]) : "v"(av), "s"(rm), "s"(as[(n) >> 4]), "i"(((n) & 15) * 64) : "memory")
  SB();
  const u32 stw = opq(L_RED + w * 6144 + (lane >> 2) * 64 + (((lane & 3) ^ ((lane >> 5) * 3)) << 4));
  const u32 strd = L_RED + w * 6144 + t16 * 64;
  const u32 gx = (t16 >> 3) * 3;

  const u32 xrd = opq(strd + ((sq ^ gx) << 4));
  f2 X[16][4];
  v4u xres[4];
  const u32 rmask = im - 1u;
  f2 s1 = {0.f, 0.f}, s2 = {0.f, 0.f};
  v4u F[16];
#pragma unroll
  for (int s = 0; s < 18; ++s) {
    if (s < 16) {
      asm volatile("s_waitcnt vmcnt(%1)" : "+v"(XR[s]) : "i"(31 + 2 * PFA) : "memory");
      if (s == 0) STAMPX(2, 4); if (s == 4) STAMPX(2, 5); if (s == 8) STAMPX(2, 6); if (s == 15) STAMPX(2, 7);
      stl<v4u>(stw, (s % 6) * 1024, XR[s]);
      F[s] = ldl<v4u>(xrd, (s % 6) * 1024);
      ldW(16 + s);
      SB();
    }
    if (s >= 2) {
      const int u = s - 2;
      if (u < 4) xres[u] = F[u] & rmask;
#pragma unroll
      for (int j = 0; j < 4; ++j) { X[u][j] = unpk(F[u][j]); s1 += X[u][j]; s2 += X[u][j] * X[u][j]; }
      asm volatile("" : "+v"(X[u][0]), "+v"(X[u][1]), "+v"(X[u][2]), "+v"(X[u][3]), "+v"(s1), "+v"(s2));
      SB();
    }
  }
  STAMP(1); STAMPX(2, 8);
  float mean = xsum32(xsum16(s1[0] + s1[1])) * (1.f / 512), var = xsum32(xsum16(s2[0] + s2[1])) * (1.f / 512) - mean * mean;
  float rstd = __builtin_amdgcn_rsqf(fmaxf(var, 0.f) + 1e-5f);
  VM(24 + 2 * PFA);
  bar();
  STAMP(2); STAMPX(2, 9);
  {
    const v4u sent = {SENT, SENT, SENT, SENT};
    gemm1<0>(k, X, rstd, -mean * rstd, [&](auto ic) __attribute__((always_inline)) {
      constexpr int n = ic;
      if constexpr (cntA(n) > cntA(n - 1)) LDA(cntA(n - 1));
      constexpr int u = (n & 1) ? n / 2 : -1;
      if constexpr (u >= 0 && u < 4) st16c(k.ws, k.l16, O_PQ0 + (me * 16 + w * 4 + u) * 1024, sent);
      if constexpr (u >= 4 && u < 8) st16c(k.ws, k.l16, O_AB1 + (c * 512 + i * 16 + w * 4 + (u - 4)) * 1024, sent);
      if constexpr (u >= 8 && u < 12) st16c(k.ws, k.l16, O_PQ1 + (me * 16 + w * 4 + (u - 8)) * 1024, sent);
      if constexpr (u == 12) st16c(k.ws, k.l16, w < 2 ? O_CAND + (me * 2 + w) * 1024 : (w == 2 && me < SMALL_KB) ? O_SMALL + me * 1024 : WS_BYTES, sent);
      if constexpr (n == 11 || n == 27 || n == 43) {
        constexpr int young = cntA(n) + ((n + 1) / 2 < 13 ? (n + 1) / 2 : 13) + (n / 16) + (n == 11 ? 2 * PFA : 0) + 8 * (2 - n / 16);
        STAMPX(2, 10 + 2 * (n / 16));
        asm volatile("s_waitcnt vmcnt(%0)\n\ts_barrier" :: "i"(young) : "memory");
        STAMPX(2, 11 + 2 * (n / 16));
      }
    });
  }
  STAMP(3); STAMPX(2, 16);
  asm volatile("s_waitcnt vmcnt(0)" : "+v"(ep), "+v"(pfd), "+v"(pfe), "+v"(pa0), "+v"(pa1) :: "memory");
  bar();
  STAMP(4); STAMPX(2, 17);
  const u64 epoch = ((u64)rfl(ep[1]) << 32) | rfl(ep[0]);
  const u64 tag64 = (epoch + 0x632BE59BD9B4E019ull) * 0x9E3779B97F4A7C15ull;
  const v2u tag = {(u32)tag64, (u32)(tag64 >> 32)};
  if (w == 0) {
    const u32 v = lane < 32 ? (lane * 8 + c) * 512 + (i >> 3) * 128 + (i & 7) * 8 : (i * 8 + (lane & 7)) * 512 + ((lane >> 3) & 3) * 128 + 64 + c * 8;
    st8c(k.ws, v, O_FLAG, tag);
  }
  SB();
#pragma unroll
  for (int n = NA1; n < 64; ++n) LDA(n);
  SB();
#define PERM(n) asm volatile("ds_write_b128 %1, %0 offset:%3\n\tds_read_b128 %0, %2 offset:%3" : "+a"(A[n]) : "v"(stw), "v"(ard), "i"(((n) % 6) * 1024) : "memory")
  const u32 ard = strd + ((q ^ gx) << 4);
#pragma unroll
  for (int n = 0; n < NA1; ++n) { PERM(n); if ((n & 3) == 3) asm volatile("s_waitcnt lgkmcnt(6)" ::: "memory"); }
  SB();
  STAMP(5); STAMPX(2, 18);
  float sumw;
  {
    const v4u wv = ld16(mkrs(wr, 1024), lane * 16, 0);
    f2 e[4];
#pragma unroll
    for (int j = 0; j < 4; ++j) e[j] = unpk(wv[j]);
    const f2 sw = (e[0] + e[1]) + (e[2] + e[3]);
    sumw = bitsf(rfl(fbits(wavesum(sw[0] + sw[1]))));
    if (w == 0) { const u32 a = opq(L_WRF + ((lane * 32 - cg * 512) & 2047)); stl<f4>(a, 0, f4{e[0][0], e[0][1], e[1][0], e[1][1]}); stl<f4>(a, 16, f4{e[2][0], e[2][1], e[3][0], e[3][1]}); }
  }
  for (int sp = 0; sp < SPIN_MAX; ++sp) {
    const v2u f = __builtin_amdgcn_raw_buffer_load_b64(k.ws, (lane & 15) * 8, O_FLAG + me * 512 + w * 128, SC1);
    asm volatile("" ::: "memory");
    if (__all(f[0] == tag[0] && f[1] == tag[1])) break;
  }
  STAMP(6); STAMPX(2, 19);
  v4u B[16];
  const u32 sb0 = O_AB0 + c * 524288 + w * 32768, sb1 = O_AB1 + c * 524288 + w * 32768;
#pragma unroll
  for (int n = 0; n < 16; ++n) { B[n] = ldb<0>(k, sb0, n); SB(); }
#pragma unroll
  for (int n = NA1; n < 64; ++n) { PERM(n); if ((n & 3) == 3) asm volatile("s_waitcnt lgkmcnt(6)" ::: "memory"); }
  bar();
  STAMP(7); STAMPX(2, 20);

  v4u PQ[16][2];
  const u32 pqs = O_PQ0 + (i * 4 + w) * 32768;
  auto ldpq = [&](int s, int m) __attribute__((always_inline)) { return ld16c(k.ws, k.l16 + (s & 3) * 1024, pqs + m * 16384 + (((s >> 2) + cg) & 3) * 4096); };
  gemm2<0>(k, A, B, sb0, xres, [&](auto ic) __attribute__((always_inline)) {
    constexpr int n = ic;
    if constexpr (n >= 112 && n < 120) { constexpr int u = n - 112, s = (u & 3) * 4 + (u >> 2); PQ[s][0] = ldpq(s, 0); PQ[s][1] = ldpq(s, 1); }
  });
  STAMP(8);
  const bool vA = r5 < 28;
  const u32 jA = x8 * 28 + (vA ? r5 : 0), jB = x8 * 32 + r5;
  v4u GU[4]; u32 Dw[8];
  {
    const rsrc_t rg = mkrs(wg, 512 * 3584), ru = mkrs(wu, 512 * 3584);
    GU[0] = ld16n(rg, tid * 7168, jA * 16); GU[1] = ld16n(rg, tid * 7168 + 3584, jA * 16);
    GU[2] = ld16n(ru, tid * 7168, jA * 16); GU[3] = ld16n(ru, tid * 7168 + 3584, jA * 16);
  }
  SB();
  f2 sw = {0.f, 0.f};
  s1 = f2{0.f, 0.f}; s2 = f2{0.f, 0.f};
  const u32 wra = opq(L_WRF + sq * 32);
  f4 wq[2][2];
  auto ldwq = [&](int s, int p) __attribute__((always_inline)) { wq[p][0] = ldl<f4>(wra, s * 128); wq[p][1] = ldl<f4>(wra, s * 128 + 16); };
  auto take = [&](int s, int p) __attribute__((always_inline)) {
    const f4 w0 = wq[p][0], w1 = wq[p][1];
    const f2 wf[4] = {f2{w0[0], w0[1]}, f2{w0[2], w0[3]}, f2{w1[0], w1[1]}, f2{w1[2], w1[3]}};
#pragma unroll
    for (int j = 0; j < 4; ++j) { X[s][j] = unpk(PQ[s][0][j]) - unpk(PQ[s][1][j]); s1 += X[s][j]; s2 += X[s][j] * X[s][j]; sw += X[s][j] * wf[j]; }
    asm volatile("" : "+v"(X[s][0]), "+v"(X[s][1]), "+v"(X[s][2]), "+v"(X[s][3]), "+v"(s1), "+v"(s2), "+v"(sw));
  };
  auto ldq = [&](int r) __attribute__((always_inline)) {
#pragma unroll
    for (int s = r; s < 16; s += 4) { PQ[s][0] = ldpq(s, 0); PQ[s][1] = ldpq(s, 1); }
  };
  auto badq = [&](int r) __attribute__((always_inline)) {
    u32 mx = 0;
#pragma unroll
    for (int s = r; s < 16; s += 4) mx = umax(mx, umax(max4(PQ[s][0]), max4(PQ[s][1])));
    return __any(mx == SENT);
  };
  auto pollq = [&](int r) __attribute__((always_inline)) {
    for (int sp = 0; sp < SPIN_MAX; ++sp) { ldq(r); asm volatile("" ::: "memory"); if (!badq(r)) break; }
  };
  auto takeq = [&](int r) __attribute__((always_inline)) {
#pragma unroll
    for (int u = 0; u < 4; ++u) { if (u < 3) ldwq(4 * u + 4 + r, (u + 1) & 1); else if (r < 3) ldwq(r + 1, 0); SB(); take(4 * u + r, u & 1); SB(); }
  };
  ldq(2);
  ldwq(0, 0);
  SB();
  takeq(0); takeq(1);
  STAMP(9);
  if (badq(2)) pollq(2);
  ldq(3);
  SB();
  takeq(2);
  if (badq(3)) pollq(3);
  STAMP(10);
  {
    const rsrc_t rd = mkrs(wd, 1792 * 1024);
#pragma unroll
    for (int j = 0; j < 8; ++j) Dw[j] = ld4n(rd, tid * 8192 + j * 1024, jB * 4);
  }
  SB();
  takeq(3);
  if (__builtin_expect(__any(s1[0] + s1[1] != s1[0] + s1[1]), 0)) {
    s1 = f2{0.f, 0.f}; s2 = f2{0.f, 0.f}; sw = f2{0.f, 0.f};
#pragma unroll
    for (int s = 0; s < 16; ++s) {
      PQ[s][0] = poll16(k.ws, k.l16 + (s & 3) * 1024, pqs + (((s >> 2) + cg) & 3) * 4096);
      PQ[s][1] = poll16(k.ws, k.l16 + (s & 3) * 1024, pqs + 16384 + (((s >> 2) + cg) & 3) * 4096);
      ldwq(s, 0); take(s, 0);
    }
  }
  mean = xsum32(xsum16(s1[0] + s1[1])) * (1.f / 512); var = fmaxf(xsum32(xsum16(s2[0] + s2[1])) * (1.f / 512) - mean * mean, 0.f);
  rstd = __builtin_amdgcn_rsqf(var + 1e-5f);
  if (me == 0 && tid == 0) st8c(k.ws, 0, O_EPOCH, v2u{(u32)(epoch + 1), (u32)((epoch + 1) >> 32)});
  if (!(c & 1) && w == (c >> 1)) {
    const float r = (xsum32(xsum16(sw[0] + sw[1])) - mean * sumw) * rstd;
    const u32 key = mkkey(r, 64 * i + 16 * w + t16), best = rowmax(key);
    const int src = __builtin_ctzll(__ballot(key == best));
    const v4u rec = {best, san(__builtin_amdgcn_readlane(fbits(r), src)), san(__builtin_amdgcn_readlane(fbits(mean), src)), san(__builtin_amdgcn_readlane(fbits(rstd), src))};
    if (lane < 8) st16c(k.ws, lane * 2048, O_KEY1 + (i * 4 + w) * 16, rec);
  }
  const float g1 = 1.f + rstd, s3 = g1 * __builtin_amdgcn_rsqf(var * g1 * g1 + 1e-5f);
#pragma unroll
  for (int s = 0; s < 4; ++s)
#pragma unroll
    for (int j = 0; j < 4; ++j) xres[s][j] = pk(X[s][j] * g1 - mean * rstd) & rmask;
  asm volatile("" : "+v"(xres[0]), "+v"(xres[1]), "+v"(xres[2]), "+v"(xres[3]));
  STAMP(11);
  u32 gate;
  const u32 gv = (lane >> 2) * 2048 + (lane & 3) * 512 + 496;
  gemm1<1>(k, X, s3, -mean * s3, [&](auto ic) __attribute__((always_inline)) {
    constexpr int n = ic;
    if constexpr (n == 40) gate = ld4c(k.ws, gv, sb1);
    if constexpr (n == 52) {
      if (__any(gate == SENT)) poll4(k.ws, gv, sb1);
#pragma unroll
      for (int u = 0; u < 16; ++u) { B[u] = ldb<1>(k, sb1, u); SB(); }
    }
  });
  STAMP(12);
  v4u K0, K1, HH, R0, R1, YY;
  u32 zp, zq, idx1 = 0; float r1 = 0.f, m1 = 0.f, rs1 = 0.f;
  v4u P5[2][2], DL[2][2][2], WV[2]; u32 cs = 0, ss = 0;
  const u32 p5s = O_PQ1 + me * 16384 + w * 2048;
  gemm2<1>(k, A, B, sb1, xres, [&](auto ic) __attribute__((always_inline)) {
    constexpr int n = ic;
    if constexpr (n == 0) { K0 = ld16c(k.ws, k.l16, O_KEY1 + k.rep * 2048); K1 = ld16c(k.ws, k.l16 + 1024, O_KEY1 + k.rep * 2048); }
    if constexpr (n == 10) {
      if (__any(umax(max4(K0), max4(K1)) == SENT)) { K0 = poll16(k.ws, k.l16, O_KEY1 + k.rep * 2048); K1 = poll16(k.ws, k.l16 + 1024, O_KEY1 + k.rep * 2048); }
      const v4u kk = K0[0] > K1[0] ? K0 : K1;
      const u32 best = wavemax(kk[0]);
      const int src = __builtin_ctzll(__ballot(kk[0] == best));
      idx1 = 2047u - (rfl(best) & 2047u);
      r1 = bitsf(__builtin_amdgcn_readlane(kk[1], src)); m1 = bitsf(__builtin_amdgcn_readlane(kk[2], src)); rs1 = bitsf(__builtin_amdgcn_readlane(kk[3], src));
      const u32 ch = (tid >> 2) & 3, v = (tid >> 4) * 1024 + (((ch & 1) << 1) | (ch >> 1)) * 256 + (tid & 3) * 4;
      zp = ld4c(k.ws, v, O_PQ0 + (idx1 >> 4) * 32768 + (idx1 & 15) * 16); zq = ld4c(k.ws, v + 16384, O_PQ0 + (idx1 >> 4) * 32768 + (idx1 & 15) * 16);
    }
    if constexpr (n == 22) {
      if (__any(umax(zp, zq) == SENT)) {
        const u32 ch = (tid >> 2) & 3, v = (tid >> 4) * 1024 + (((ch & 1) << 1) | (ch >> 1)) * 256 + (tid & 3) * 4;
        zp = poll4(k.ws, v, O_PQ0 + (idx1 >> 4) * 32768 + (idx1 & 15) * 16); zq = poll4(k.ws, v + 16384, O_PQ0 + (idx1 >> 4) * 32768 + (idx1 & 15) * 16);
      }
      const f2 z = unpk(pk((unpk(zp) - unpk(zq) - m1) * rs1));
      gemv1_part(k, z[0], z[1], GU);
    }
    if constexpr (n == 27) { if (w == 0) gemv1_fin(k, O_H1, jA, vA); }
    if constexpr (n == 48) HH = ld16c(k.ws, tid * 16, O_H1 + k.rep * 4096);
    if constexpr (n == 60) {
      if (__any(tid < 224 && max4(HH) == SENT)) HH = poll16(k.ws, tid * 16, O_H1 + k.rep * 4096, tid < 224);
      gemv2_part(k, HH, Dw);
    }
    if constexpr (n == 71) { if (w == 0) { const f2 y = gemv2_fin(r1); if (lane < 8) st4c(k.ws, lane * 2048, O_Y1 + jB * 4, san(pk(y))); } }
    if constexpr (n == 92) {
      const u32 ch = lane & 3, v = (lane >> 2) * 1024 + (((ch & 1) << 1) | (ch >> 1)) * 256;
      R0 = ld16c(k.ws, v, O_PQ0 + (idx1 >> 4) * 32768 + (idx1 & 15) * 16); R1 = ld16c(k.ws, v + 16384, O_PQ0 + (idx1 >> 4) * 32768 + (idx1 & 15) * 16);
      YY = ld16c(k.ws, k.l16, O_Y1 + k.rep * 2048);
    }
    if constexpr (n == 104) {
      if (__any(umax(max4(YY), umax(max4(R0), max4(R1))) == SENT)) {
        const u32 ch = lane & 3, v = (lane >> 2) * 1024 + (((ch & 1) << 1) | (ch >> 1)) * 256;
        R0 = poll16(k.ws, v, O_PQ0 + (idx1 >> 4) * 32768 + (idx1 & 15) * 16); R1 = poll16(k.ws, v + 16384, O_PQ0 + (idx1 >> 4) * 32768 + (idx1 & 15) * 16);
        YY = poll16(k.ws, k.l16, O_Y1 + k.rep * 2048);
      }
      const float ga = 1.f + rs1, va = fmaxf(1.f / (rs1 * rs1) - 1e-5f, 0.f), s3o = ga * __builtin_amdgcn_rsqf(va * ga * ga + 1e-5f);
      f2 xn[4], zo[4], a1 = {0.f, 0.f}, a2 = {0.f, 0.f};
      const u32 dxa = opq(L_DX + lane * 32);
#pragma unroll
      for (int j = 0; j < 4; ++j) {
        const f2 x1 = unpk(R0[j]) - unpk(R1[j]);
        const f2 z2 = unpk(pk((x1 - m1) * rs1));
        xn[j] = unpk(pk(x1 + unpk(pk(z2 + unpk(YY[j])))));
        zo[j] = unpk(pk(x1 * s3o - m1 * s3o));
        const f2 dx = xn[j] - unpk(pk(x1 * ga - m1 * rs1));
        if (w == 0) stl<f2>(dxa, j * 8, dx);
        a1 += xn[j]; a2 += xn[j] * xn[j];
      }
      const float mn = wavesum(a1[0] + a1[1]) * (1.f / 512), vn = fmaxf(wavesum(a2[0] + a2[1]) * (1.f / 512) - mn * mn, 0.f), rn = __builtin_amdgcn_rsqf(vn + 1e-5f);
      const u32 e = 4 * i + w;
      const v4u wv = ldl<v4u>(L_W + e * 1024 + ((((lane >> 4) - cg) & 3) * 16 + ((lane ^ e) & 15)) * 16);
      f2 d = {0.f, 0.f};
#pragma unroll
      for (int j = 0; j < 4; ++j) d += (unpk(pk((xn[j] - mn) * rn)) - zo[j]) * unpk(wv[j]);
      const float dl = wavesum(d[0] + d[1]);
      if (lane < 8) st4c(k.ws, lane * 4096, O_DEL + (128 * c + e) * 4, san(fbits(dl)));
    }
    if constexpr (n == 114) {
      P5[0][0] = ld16c(k.ws, k.l16, p5s); P5[0][1] = ld16c(k.ws, k.l16, p5s + 8192);
      const rsrc_t rr = mkrs(wr, 1024);
      WV[0] = ld16(rr, (lane >> 3) * 16, w * 256); WV[1] = ld16(rr, (lane >> 3) * 16 + 128, w * 256);
      const u32 v = (64 * i + 8 * c + (lane & 7)) * 4096;
      cs = __builtin_amdgcn_raw_buffer_load_b16(mkrs(Cs, 2048 * 4096), v, idx1 * 2, 0); ss = __builtin_amdgcn_raw_buffer_load_b16(mkrs(Ss, 2048 * 4096), v, idx1 * 2, 0);
    }
  });
  STAMP(13);
  const u32 dv = opq((lane >> 3) * 32), dso = O_DEL + k.rep * 4096 + w * 512;
  for (int sp = 0; sp < SPIN_MAX; ++sp) {
    u32 mx = 0;
#pragma unroll
    for (int n = 0; n < 2; ++n)
#pragma unroll
      for (int m = 0; m < 2; ++m)
#pragma unroll
        for (int h = 0; h < 2; ++h) { DL[n][m][h] = ld16c(k.ws, dv + n * 256 + h * 16 + m * 2048, dso); }
    asm volatile("" ::: "memory");
#pragma unroll
    for (int n = 0; n < 2; ++n)
#pragma unroll
      for (int m = 0; m < 2; ++m) mx = umax(mx, umax(max4(DL[n][m][0]), max4(DL[n][m][1])));
    if (!__any(mx == SENT)) break;
  }
  if (__any(umax(max4(P5[0][0]), max4(P5[0][1])) == SENT)) { P5[0][0] = poll16(k.ws, k.l16, p5s); P5[0][1] = poll16(k.ws, k.l16, p5s + 8192); }
  STAMP(14);
  for (int sp = 0; sp < SPIN_MAX; ++sp) {
    P5[1][0] = ld16c(k.ws, k.l16 + 1024, p5s); P5[1][1] = ld16c(k.ws, k.l16 + 1024, p5s + 8192);
    asm volatile("" ::: "memory");
    if (!__any(umax(max4(P5[1][0]), max4(P5[1][1])) == SENT)) break;
  }
  STAMP(15);
  const u32 row = 64 * i + 8 * c + (lane & 7);
  f2 Y[2][4];
  {
    const float fc = blo(cs), fs = blo(ss);
    const u32 dxr = opq(L_DX + (16 * w + (lane >> 3)) * 32);
    s1 = f2{0.f, 0.f}; s2 = f2{0.f, 0.f}; sw = f2{0.f, 0.f};
#pragma unroll
    for (int n = 0; n < 2; ++n)
#pragma unroll
      for (int j = 0; j < 4; ++j) {
        const f2 da = {bitsf(DL[n][0][j >> 1][2 * (j & 1)]), bitsf(DL[n][0][j >> 1][2 * (j & 1) + 1])}, db = {bitsf(DL[n][1][j >> 1][2 * (j & 1)]), bitsf(DL[n][1][j >> 1][2 * (j & 1) + 1])};
        f2 v = unpk(P5[n][0][j]) - unpk(P5[n][1][j]) + (da * fc - db * fs);
        const f2 dx = ldl<f2>(dxr, n * 256 + j * 8);
        if (row == idx1) v += dx;
        v = unpk(pk(v));
        Y[n][j] = v; s1 += v; s2 += v * v; sw += v * unpk(WV[n][j]);
      }
    float a = s1[0] + s1[1], bb = s2[0] + s2[1], cc = sw[0] + sw[1];
    a += dppf<0x128>(a); bb += dppf<0x128>(bb); cc += dppf<0x128>(cc);
    a = xsum32(xsum16(a)); bb = xsum32(xsum16(bb)); cc = xsum32(xsum16(cc));
    if (lane < 8) stl<f4>(opq(L_ST + (w * 8 + lane) * 16), 0, f4{a, bb, cc, 0.f});
  }
  bar();
  const u32 sta = opq(L_ST + (lane & 7) * 16);
  const f4 st4 = (ldl<f4>(sta) + ldl<f4>(sta, 128)) + (ldl<f4>(sta, 256) + ldl<f4>(sta, 384));
  mean = st4[0] * (1.f / 512); var = fmaxf(st4[1] * (1.f / 512) - mean * mean, 0.f); rstd = __builtin_amdgcn_rsqf(var + 1e-5f);
  const float r4 = (st4[2] - mean * sumw) * rstd;
  const u32 key = mkkey(r4, row);
  u32 best = umax(key, dppu<0x141>(key)); best = umax(best, dppu<0xB1>(best)); best = umax(best, dppu<0x4E>(best));
  const bool mine = key == best;
  if (w == 0) {
    const int src = __builtin_ctzll(__ballot(mine));
    if (lane < 8) st8c(k.ws, lane * 2048, O_KEY2 + me * 8, v2u{best, san(__builtin_amdgcn_readlane(fbits(r4), src))});
  }
  const rsrc_t ro = mkrs(out, 2048 * 1024);
  v4u O[2];
#pragma unroll
  for (int n = 0; n < 2; ++n) {
    v4u z, xx;
#pragma unroll
    for (int j = 0; j < 4; ++j) { z[j] = san(pk((Y[n][j] - mean) * rstd)); xx[j] = san(pk(Y[n][j])); O[n][j] = pk(Y[n][j] + unpk(z[j])); }
    const u32 cb = (16 * w + 8 * n + (lane >> 3)) * 16;
    if (mine) { st16c(k.ws, cb, O_CAND + me * 2048, z); st16c(k.ws, cb + 1024, O_CAND + me * 2048, xx); }
    else __builtin_amdgcn_raw_buffer_store_b128(O[n], ro, row * 1024 + cb, 0, 0);
  }
  STAMP(16);
  if (w == 0) {
    v4u a, bb;
    for (int sp = 0; sp < SPIN_MAX; ++sp) {
      a = ld16c(k.ws, k.l16, O_KEY2 + k.rep * 2048); bb = ld16c(k.ws, k.l16 + 1024, O_KEY2 + k.rep * 2048);
      asm volatile("" ::: "memory");
      if (!__any(umax(max4(a), max4(bb)) == SENT)) break;
    }
    u32 kk = a[0], rr = a[1];
    if (a[2] > kk) { kk = a[2]; rr = a[3]; }
    if (bb[0] > kk) { kk = bb[0]; rr = bb[1]; }
    if (bb[2] > kk) { kk = bb[2]; rr = bb[3]; }
    const u32 gb = wavemax(kk);
    if (kk == gb) stl<v2u>(opq(L_BC), 0, v2u{gb, rr});
  }
  bar();
  STAMP(17);
  const v2u gb = ldl<v2u>(opq(L_BC));
  const u32 idx2 = 2047u - (rfl(gb[0]) & 2047u);
  const u32 cso = O_CAND + (idx2 >> 3) * 2048;
  u32 zz = ld4c(k.ws, tid * 4, cso), fz = ld4c(k.ws, 0, cso + jB * 4), fx = ld4c(k.ws, 1024, cso + jB * 4);
  if (mine && row != idx2) {
#pragma unroll
    for (int n = 0; n < 2; ++n) __builtin_amdgcn_raw_buffer_store_b128(O[n], ro, row * 1024 + (16 * w + 8 * n + (lane >> 3)) * 16, 0, 0);
  }
  if (__any(zz == SENT)) zz = poll4(k.ws, tid * 4, cso);
  gemv1_part(k, blo(zz), bhi(zz), GU);
  bar();
  if (w == 0) gemv1_fin(k, O_H2, jA, vA);
  STAMP(18);
  HH = poll16(k.ws, tid * 16, O_H2 + k.rep * 4096, tid < 224);
  STAMP(19);
  gemv2_part(k, HH, Dw);
  bar();
  if (w == 0) {
    if (__any(umax(fz, fx) == SENT)) { fz = poll4(k.ws, 0, cso + jB * 4); fx = poll4(k.ws, 1024, cso + jB * 4); }
    const f2 y = gemv2_fin(bitsf(gb[1]));
    const u32 o = pk(unpk(fx) + unpk(pk(unpk(fz) + y)));
    if (lane == 0) __builtin_amdgcn_raw_buffer_store_b32(o, ro, idx2 * 1024 + jB * 4, 0, 0);
  }
  STAMP(20);
  asm volatile("fm_code_end:");
}

extern "C" void kernel_launch(void* const* d_in, const int* in_sizes, int n_in, void* d_out, int out_size, void* d_ws, size_t ws_size, hipStream_t stream) {
  (void)hipFuncSetAttribute((const void*)fnet_mod_kernel, hipFuncAttributeMaxDynamicSharedMemorySize, LDS_BYTES);
  fnet_mod_kernel<<<256, 256, LDS_BYTES, stream>>>((const u16*)d_in[0], (const u16*)d_in[1], (const u16*)d_in[2], (const u16*)d_in[3], (const u16*)d_in[4], (const u16*)d_in[6],
                                                   (const u16*)d_in[7], (const u16*)d_in[8], (const u16*)d_in[9], (u16*)d_out, (unsigned char*)d_ws + (WS_END ? ((ws_size - WS_BYTES) & ~(size_t)4095) : 0));
}
```

```cpp
#define KLN 44
#define KB 58
#define GLN 48
#define FC 50
#include <hip/hip_runtime.h>
#include <hip/hip_bf16.h>
#include <stdint.h>
#include <utility>

typedef unsigned u32;
typedef unsigned long long u64;
typedef unsigned short u16;
typedef u32 v4u __attribute__((ext_vector_type(4)));
typedef u32 v2u __attribute__((ext_vector_type(2)));
typedef float f2 __attribute__((ext_vector_type(2)));
typedef float f4 __attribute__((ext_vector_type(4)));
typedef __bf16 bf2 __attribute__((ext_vector_type(2)));
typedef __amdgpu_buffer_rsrc_t rsrc_t;
#define DI __device__ __forceinline__
#define AI __attribute__((always_inline))
#define LAS __attribute__((address_space(3)))
#define SB() __builtin_amdgcn_sched_barrier(0)
#define PIN(x) asm volatile("" : "+v"(x))
#define PIN2(x, y) asm volatile("" : "+v"(x), "+v"(y))
#define SPIN _Pragma("unroll 1") for (int sp = 0; sp < SPIN_MAX; ++sp)

#define WIN 16
#define NOINIT 0
#define NOPF 0
#define NOW1 0
#define NJ 8
#define FB 36
#define ROT 0
#define SL1 22
#define SL2 10
#define SL0 0
#define XP 0
#define Q3E 0
#define SLP 6
#define WX 12
#define WG 1
#define PFB 1
#define KL 16
#define OWN 0
#define O0 4
#define OS 3
#define OW0 0
#define PFW 0
#define PFE 2
#define LAG 16
#define HOLD2 1
#define PT1 1
#define PTS 4
#define KSL 8
#define NPQ 10
#define PX0 10
#define PXS 1
#define PXN 6
#define PF0 20
#define PFS 1
#define PAT0 28
#define PATS 1
#define FLU 12
#define WU 48
#define NPRE 32

constexpr u32 SENT = 0xFFFFFFFFu;
constexpr int SC1 = 16;
constexpr int SPIN_MAX = 200000;
constexpr u32 O_EPOCH = 0, O_FLAG = 4096, O_FLAG2 = O_FLAG + 131072;
constexpr u32 O_SM1 = 0x80000;
constexpr u32 O_KEY1 = O_SM1, O_Y1 = O_SM1 + 16384, O_DEL = O_SM1 + 32768, O_YP1 = O_SM1 + 65536;
constexpr u32 O_SM2 = 0x100000;
constexpr u32 O_CAND = O_SM2, O_KEY2 = O_SM2 + 524288, O_YP2 = O_KEY2 + 16384;
static_assert(O_YP1 + 64 * 7168 == O_SM2 && O_YP2 + 64 * 7168 + 1024 <= O_SM2 + 0x100000, "small regions");
constexpr u32 O_FLAG4 = 2u << 20;
constexpr u32 O_AB0 = 3u << 20, O_AB1 = 7u << 20, O_PQ0 = 11u << 20, O_PQ1 = 15u << 20, O_GT = 19u << 20, WS_BYTES = 23u << 20;
constexpr u32 O_DBG = 32u << 20;
constexpr u32 L_W = 0, L_RED = 131072, L_OW = 155648, L_YX = 157696, L_ZS = 158720, L_P1 = 159744, L_P2 = 160000, L_ST = 160128, L_BC = 160640, L_W1 = L_YX, L_W1S = 160768,
              L_DBG = 162816, LDS_BYTES = 163840;

DI rsrc_t mkrs(const void* p, u32 bytes) { return __builtin_amdgcn_make_buffer_rsrc((void*)p, 0, bytes, 0x00020000); }
DI v4u ld16(rsrc_t r, u32 v, u32 s) { return __builtin_amdgcn_raw_buffer_load_b128(r, v, s, 0); }
DI u32 ld4(rsrc_t r, u32 v, u32 s) { return __builtin_amdgcn_raw_buffer_load_b32(r, v, s, 0); }
DI v4u ld16c(rsrc_t r, u32 v, u32 s) { return __builtin_amdgcn_raw_buffer_load_b128(r, v, s, SC1); }
DI v2u ld8c(rsrc_t r, u32 v, u32 s) { return __builtin_amdgcn_raw_buffer_load_b64(r, v, s, SC1); }
DI u32 ld4c(rsrc_t r, u32 v, u32 s) { return __builtin_amdgcn_raw_buffer_load_b32(r, v, s, SC1); }
template <int AUX> DI void st16(rsrc_t r, u32 v, u32 s, v4u d) { __builtin_amdgcn_raw_buffer_store_b128(d, r, v, s, AUX); asm volatile("s_nop 1" :: "v"(d) : "memory"); }
DI void st16c(rsrc_t r, u32 v, u32 s, v4u d) { st16<SC1>(r, v, s, d); }
DI void st8c(rsrc_t r, u32 v, u32 s, v2u d) { __builtin_amdgcn_raw_buffer_store_b64(d, r, v, s, SC1); }
DI void st4c(rsrc_t r, u32 v, u32 s, u32 d) { __builtin_amdgcn_raw_buffer_store_b32(d, r, v, s, SC1); }
DI float blo(u32 d) { return __builtin_bit_cast(float, d << 16); }
DI float bhi(u32 d) { return __builtin_bit_cast(float, d & 0xffff0000u); }
DI f2 unpk(u32 d) { return f2{blo(d), bhi(d)}; }
DI u32 pk(f2 v) { return __builtin_bit_cast(u32, __builtin_convertvector(v, bf2)); }
DI float rbf(float v) { return blo(pk(f2{v, 0.f})); }
DI u32 san(u32 d) { return d < SENT ? d : SENT - 1u; }
DI u32 fbits(float f) { return __builtin_bit_cast(u32, f); }
DI float bitsf(u32 u) { return __builtin_bit_cast(float, u); }
DI u32 umax(u32 a, u32 b) { return a > b ? a : b; }
DI u32 max4(v4u a) { return umax(umax(a[0], a[1]), umax(a[2], a[3])); }
template <int C> DI float dppf(float x) { return bitsf(__builtin_amdgcn_mov_dpp((int)fbits(x), C, 0xf, 0xf, true)); }
template <int C> DI u32 dppu(u32 x) { return (u32)__builtin_amdgcn_mov_dpp((int)x, C, 0xf, 0xf, true); }
DI float xsum16(float x) { auto s = __builtin_amdgcn_permlane16_swap(fbits(x), fbits(x), false, false); return bitsf(s[0]) + bitsf(s[1]); }
DI float xsum32(float x) { auto s = __builtin_amdgcn_permlane32_swap(fbits(x), fbits(x), false, false); return bitsf(s[0]) + bitsf(s[1]); }
DI float rowsum(float x) { x += dppf<0x128>(x); x += dppf<0x124>(x); x += dppf<0x122>(x); x += dppf<0x121>(x); return x; }
DI float wavesum(float x) { return xsum32(xsum16(rowsum(x))); }
DI u32 rowmax(u32 x) { x = umax(x, dppu<0x128>(x)); x = umax(x, dppu<0x124>(x)); x = umax(x, dppu<0x122>(x)); x = umax(x, dppu<0x121>(x)); return x; }
DI u32 wavemax(u32 x) {
  x = rowmax(x);
  auto s = __builtin_amdgcn_permlane16_swap(x, x, false, false); x = umax(s[0], s[1]);
  auto t = __builtin_amdgcn_permlane32_swap(x, x, false, false); return umax(t[0], t[1]);
}
DI u32 mkkey(float r, u32 row) { u32 u = fbits(r); u = (u & 0x80000000u) ? ~u : (u | 0x80000000u); return san((u & ~2047u) | (2047u - row)); }
DI u32 rfl(u32 x) { return __builtin_amdgcn_readfirstlane(x); }
DI u32 opq(u32 x) { asm volatile("" : "+v"(x)); return x; }
DI v4u mksent() { v4u v = {SENT, SENT, SENT, SENT}; PIN(v); return v; }
DI void bar() { asm volatile("s_waitcnt lgkmcnt(0)\n\ts_barrier" ::: "memory"); }
template <typename T> DI T ldl(u32 a, u32 o = 0) { return *(const LAS T*)((const LAS char*)(size_t)a + o); }
template <typename T> DI void stl(u32 a, u32 o, T v) { *(LAS T*)((LAS char*)(size_t)a + o) = v; }
#define MFMA0(acc, a, b, CA, CB) asm volatile("v_mfma_f32_16x16x32_bf16 %0, %1, %2, 0" : "=&v"(acc) : CA(a), CB(b))
#define MFMAC(acc, a, b, c, CA, CB) asm volatile("v_mfma_f32_16x16x32_bf16 %0, %1, %2, %3" : "=&v"(acc) : CA(a), CB(b), "v"(c))
#define MFMA(acc, a, b, CA, CB) asm volatile("v_mfma_f32_16x16x32_bf16 %0, %1, %2, %0" : "+v"(acc) : CA(a), CB(b))
#define VM(n) asm volatile("s_waitcnt vmcnt(%0)" :: "i"(n) : "memory")
template <int N> using IC = std::integral_constant<int, N>;
template <int... I, typename F> DI void sfor_(std::integer_sequence<int, I...>, F&& f) { (f(IC<I>{}), ...); }
template <int N, typename F> DI void sfor(F&& f) { sfor_(std::make_integer_sequence<int, N>{}, f); }

#define STAMP(n)
#define STAMPX(set, n)
#define STAMPC(n)
#define WAITT(n, ...) do { __VA_ARGS__; } while (0)

struct Ctx {
  rsrc_t ws;
  u32 lane, l16, w, tid, i, c, cg, im, rep, rot;
};

constexpr int wtop(int n) { const int t = n + 7; return t < 63 ? t : 63; }
template <int L, typename HOOK>
DI void gemm1(const Ctx& k, const v4u (&Z)[16], HOOK&& hook) {
  v4u held;
  constexpr int NR = L ? 16 : 24;
  v4u Wf[NR];
  f4 acc[2][2], w1a[2][2], so[2], pw[2];
  const u32 ln = opq(k.lane), t16 = ln & 15, q = ln >> 4, sq = ((q & 1) << 1) | (q >> 1);
  u32 wb[8];
#pragma unroll
  for (int h = 0; h < 8; ++h) wb[h] = opq(L_W + (h >> 2) * 65536 + t16 * 1024 + (((4 * (h & 3) + sq) ^ t16) << 4));
  const u32 abv = (ln >> 5) * 256 + (q & 1) * 1024 + t16 * 16;
  const u32 abs0 = (L ? O_AB1 : O_AB0) + k.c * 524288 + (2 * k.i + (k.w >> 1)) * 2048 + (k.w & 1) * 512;
  const u32 w1r = opq(L_W1 + t16 * 16), w1s = opq(L_W1S + t16 * 4);
  const v4u ones = {0x3F803F80u, 0x3F803F80u, 0x3F803F80u, 0x3F803F80u};
  auto ldw = [&](int n) AI {
    const int qd = n >> 4, s = n & 15;
#pragma unroll
    for (int j = 1; j >= 0; --j) { const int ct = 2 * qd + j; Wf[(2 * n + j) % NR] = ldl<v4u>(wb[(ct >> 2) * 4 + (s & 3)], (ct & 3) * 16384 + (s >> 2) * 256); SB(); }
  };
  auto w1st = [&](int qd) AI { const u32 a = opq(w1r + k.w * 4); stl<float>(a, qd * 512, w1a[qd & 1][0][0]); stl<float>(a, qd * 512 + 256, w1a[qd & 1][1][0]); };
  auto finr = [&](int qd) AI {
    if constexpr (L == 0) { pw[0] = ldl<f4>(w1r, qd * 512); pw[1] = ldl<f4>(w1r, qd * 512 + 256); }
    else { pw[0][0] = ldl<float>(w1s, qd * 128); pw[1][0] = ldl<float>(w1s, qd * 128 + 64); }
  };
  auto fin = [&](int qd) AI {
    u32 d[2][2];
#pragma unroll
    for (int j = 0; j < 2; ++j) {
      float w1 = pw[j][0];
      if constexpr (L == 0) { w1 = (w1 + pw[j][1]) + (pw[j][2] + pw[j][3]); stl<float>(w1s, qd * 128 + j * 64, w1); }
      const f4 v = so[0] * acc[qd & 1][j] + so[1] * w1;
      d[j][0] = san(pk(f2{v[0], v[1]})); d[j][1] = san(pk(f2{v[2], v[3]}));
    }
    auto s0 = __builtin_amdgcn_permlane16_swap(d[0][0], d[1][0], false, false);
    auto s1 = __builtin_amdgcn_permlane16_swap(d[0][1], d[1][1], false, false);
    const v4u o = {s0[0], s1[0], s0[1], s1[1]};
    if (L == 1 && HOLD2 && qd == 2) { held = o; PIN(held); } else st16c(k.ws, abv, abs0 + qd * 131072, o);
    if (L == 1 && HOLD2 && qd == 3) st16c(k.ws, abv, abs0 + 2 * 131072, held);
  };
  constexpr int FR = L ? 17 : 25;
#pragma unroll
  for (int n = 0; n <= wtop(-1); ++n) ldw(n);
  if constexpr (L == 0) asm volatile("" : "=v"(w1a[0][0]), "=v"(w1a[0][1]), "=v"(w1a[1][0]), "=v"(w1a[1][1]));
  SB();
  sfor<64>([&](auto ic) AI {
    constexpr int n = ic, qd = n >> 4, s = n & 15;
    if constexpr (s == 0) { MFMA0(acc[qd & 1][0], Z[s], Wf[(2 * n) % NR], "v", "v"); MFMA0(acc[qd & 1][1], Z[s], Wf[(2 * n + 1) % NR], "v", "v"); }
    else { MFMA(acc[qd & 1][0], Z[s], Wf[(2 * n) % NR], "v", "v"); MFMA(acc[qd & 1][1], Z[s], Wf[(2 * n + 1) % NR], "v", "v"); }
    if constexpr (L == 0 && (s & 3) == 3 && !NOW1)
      asm volatile("s_cmp_eq_u32 %11, %12\n\ts_cbranch_scc0 1f\n\t"
                   "v_mfma_f32_16x16x32_bf16 %0, %2, %3, 0\n\tv_mfma_f32_16x16x32_bf16 %1, %2, %4, 0\n\tv_mfma_f32_16x16x32_bf16 %0, %2, %5, %0\n\tv_mfma_f32_16x16x32_bf16 %1, %2, %6, %1\n\t"
                   "v_mfma_f32_16x16x32_bf16 %0, %2, %7, %0\n\tv_mfma_f32_16x16x32_bf16 %1, %2, %8, %1\n\tv_mfma_f32_16x16x32_bf16 %0, %2, %9, %0\n\tv_mfma_f32_16x16x32_bf16 %1, %2, %10, %1\n1:"
                   : "+v"(w1a[qd & 1][0]), "+v"(w1a[qd & 1][1])
                   : "v"(ones), "v"(Wf[(2 * n - 6) % NR]), "v"(Wf[(2 * n - 5) % NR]), "v"(Wf[(2 * n - 4) % NR]), "v"(Wf[(2 * n - 3) % NR]), "v"(Wf[(2 * n - 2) % NR]), "v"(Wf[(2 * n - 1) % NR]),
                     "v"(Wf[(2 * n) % NR]), "v"(Wf[(2 * n + 1) % NR]), "s"(k.w), "i"(s >> 2) : "scc");
    SB();
    if constexpr (wtop(n) > wtop(n - 1)) ldw(wtop(n));
    if constexpr (L == 0 && s == 4 && qd > 0) w1st(qd - 1);
    if constexpr (n == 10) { const u32 a = opq(L_ST + k.w * 128 + q * 16); so[0] = ldl<f4>(a); so[1] = ldl<f4>(a, 64); }
    if constexpr (n >= FR && (n - FR) % 16 == 0) finr((n - FR) / 16);
    if constexpr (L == 1) hook(ic);
    if constexpr (n >= FR + 3 && (n - FR - 3) % 16 == 0) fin((n - FR - 3) / 16);
    if constexpr (L == 0) hook(ic);
    SB();
  });
  if constexpr (L == 0) {
    asm volatile("s_nop 7" : "+v"(acc[1][0]), "+v"(acc[1][1]), "+v"(w1a[1][0]), "+v"(w1a[1][1]));
    w1st(3); bar(); finr(3);
  } else {
    finr(3);
    asm volatile("s_nop 7" : "+v"(acc[1][0]), "+v"(acc[1][1]));
  }
  fin(3);
}

DI void stats(u32 lane, const f4& S, const f4& Q, float& mean, float& var) {
  const u32 t = lane & 15;
  float d = (t & 2) ? ((t & 1) ? Q[3] : Q[2]) : ((t & 1) ? Q[1] : Q[0]);
  d = (lane >> 4) == (t >> 2) ? d : 0.f;
  mean = S[0] * (1.f / 512);
  var = fmaxf(xsum32(xsum16(d)) * (1.f / 512) - mean * mean, 0.f);
}

DI v4u ldb(const Ctx& k, u32 sb, int n) { return ld16c(k.ws, k.l16 + (n & 3) * 1024, sb + (n >> 5) * 131072 + ((((n & 31) >> 2) + k.rot) & 7) * 4096); }

DI void slowpass(const Ctx& k, u32 sb, int qd, const v4u (&A)[64], f4 (&acc)[4][2], const f4& c0) {
#pragma unroll
  for (int n = 0; n < 32; ++n) {
    v4u b;
    SPIN { b = ldb(k, sb, qd * 32 + n); asm volatile("" ::: "memory"); if (!__any(max4(b) == SENT)) break; }
#pragma unroll
    for (int T = 0; T < 4; ++T) {
      if (n >= 2) MFMA(acc[T][n & 1], b, A[T * 16 + (n >> 1)], "v", "a");
      else if (T == 0) MFMAC(acc[T][n & 1], b, A[T * 16], c0, "v", "a");
      else MFMA0(acc[T][n & 1], b, A[T * 16], "v", "a");
    }
    SB();
  }
  asm volatile("s_nop 7" : "+v"(acc[0][0]), "+v"(acc[0][1]), "+v"(acc[1][0]), "+v"(acc[1][1]), "+v"(acc[2][0]), "+v"(acc[2][1]), "+v"(acc[3][0]), "+v"(acc[3][1]));
}

constexpr int NRED = 41;
constexpr int redslice(int S) {
  if (S == 4 || S == 5) return S - 4;
  if (S >= 6 && S < 24 && S % 3 == 0) return 2 + (S - 6) / 3;
  if (S >= 27 && S < 30) return S - 19;
  if (S >= 33 && S < 63) return S - 22;
  return -1;
}
constexpr int freeslot(int G) { int c = 0; for (int g = 0; g < G; ++g) if (g < 96 || redslice(g % 96) < 0) ++c; return c; }
template <int L, typename HOOK, typename SLOT, typename LAST>
DI void gemm2(const Ctx& k, v4u (&A)[64], v4u (&B)[WIN], u32 sb, const v4u (&xr)[16], float xg, float xo, HOOK&& hook, SLOT&& slot, LAST&& last) {
  f4 acc[2][4][2], r0[2], r1[2];
  float v[8], u0, u1; v4u o; u64 bad;
  const u32 ln = opq(k.lane), q = ln >> 4, t = ln & 15, sq = ((q & 1) << 1) | (q >> 1);
  const u32 pv = L ? (t >> 3) * 16384 + sq * 128 + (t & 7) * 16 : k.l16;
  const u32 ps = L ? O_PQ1 + (k.i * 8 + 2 * k.w) * 16384 + k.im * 8192 + k.cg * 2048 : O_PQ0 + ((k.i * 4 + k.w) * 2 + k.im) * 16384 + k.cg * 4096;
  f4 c0 = {xo, xo, xo, xo};
  asm volatile("s_nop 1" : "+v"(c0));
  u32 wa;
  auto red = [&](auto qc, auto jc) AI {
    constexpr int qd = qc, J = jc;
    f4 (&a)[4][2] = acc[qd & 1];
    if constexpr (J == 0 && L == 1) bad = __ballot(__builtin_isunordered(a[0][0][0], a[0][0][1])) | __ballot(__builtin_isunordered(a[0][0][2], a[0][0][3]));
    if constexpr (J == 1 && L == 1) {
      bad |= __ballot(__builtin_isunordered(a[0][1][0], a[0][1][1])) | __ballot(__builtin_isunordered(a[0][1][2], a[0][1][3]));
      if (__builtin_expect(bad != 0, 0)) WAITT(1 + qd, slowpass(k, sb, qd, A, a, c0));
    }
    if constexpr (J >= 2 && J < 8) {
      constexpr int T = (J - 2) / 2 + 1;
      if constexpr (!(J & 1)) wa = opq(k.l16 + (L_RED + ((T + k.w) & 3) * 6144 + (T - 1) * 2048));
      stl<f4>(wa, (J & 1) * 1024, a[T][J & 1]);
    }
    if constexpr (J == 8) { bar(); wa = opq(k.l16 + (L_RED + k.w * 6144)); }
    if constexpr (J == 9) { r0[0] = ldl<f4>(wa); r0[1] = ldl<f4>(wa, 1024); }
    if constexpr (J == 10) { r1[0] = ldl<f4>(wa, 2048); r1[1] = ldl<f4>(wa, 3072); }
    if constexpr (J >= 11 && J < 15) { constexpr int e = 2 * (J - 11); v[e] = a[0][e >> 2][e & 3] + r0[e >> 2][e & 3]; v[e + 1] = a[0][e >> 2][(e & 3) + 1] + r0[e >> 2][(e & 3) + 1]; PIN2(v[e], v[e + 1]); }
    if constexpr (J == 15) { r0[0] = ldl<f4>(wa, 4096); r0[1] = ldl<f4>(wa, 5120); }
    if constexpr (J >= 16 && J < 20) { constexpr int e = 2 * (J - 16); v[e] += r1[e >> 2][e & 3]; v[e + 1] += r1[e >> 2][(e & 3) + 1]; PIN2(v[e], v[e + 1]); }
    if constexpr (J >= 20 && J < 24) { constexpr int e = 2 * (J - 20); v[e] += r0[e >> 2][e & 3]; v[e + 1] += r0[e >> 2][(e & 3) + 1]; PIN2(v[e], v[e + 1]); }
    if constexpr (J >= 24 && J < 28) { constexpr int j = J - 24; auto s = __builtin_amdgcn_permlane16_swap(fbits(v[j]), fbits(v[4 + j]), false, false); v[j] = bitsf(s[0]); v[4 + j] = bitsf(s[1]); PIN2(v[j], v[4 + j]); }
    if constexpr (J >= 28 && J < 40) {
      constexpr int e = J - 28;
      constexpr int kind = e == 0 ? 0 : e == 10 ? 1 : e == 11 ? 2 : (e - 1) % 3 == 0 ? 1 : (e - 1) % 3 == 1 ? 0 : 2;
      constexpr int j = e == 0 ? 0 : e == 10 ? 3 : e == 11 ? 3 : (e - 1) / 3 + (kind == 0 ? 1 : 0);
      if constexpr (kind == 0) { u0 = blo(xr[qd][j]); u1 = bhi(xr[qd][j]); PIN2(u0, u1); }
      if constexpr (kind == 1) { v[2 * j] = __builtin_fmaf(u0, xg, v[2 * j]); v[2 * j + 1] = __builtin_fmaf(u1, xg, v[2 * j + 1]); PIN2(v[2 * j], v[2 * j + 1]); }
      if constexpr (kind == 2) { o[j] = san(pk(f2{v[2 * j], v[2 * j + 1]})); PIN(o[j]); }
    }
    if constexpr (J == 40) st16c(k.ws, pv, ps + qd * (L ? 512 : 1024), o);
  };
  sfor<128>([&](auto ic) AI {
    constexpr int n = ic, qd = n >> 5, m = n & 31;
    sfor<4>([&](auto tc) AI {
      constexpr int T = tc;
      if constexpr (m >= 2) MFMA(acc[qd & 1][T][m & 1], B[n % WIN], A[T * 16 + (m >> 1)], "v", "a");
      else if constexpr (T == 0 && L == 1) MFMAC(acc[qd & 1][T][m & 1], B[n % WIN], A[T * 16], c0, "v", "a");
      else MFMA0(acc[qd & 1][T][m & 1], B[n % WIN], A[T * 16], "v", "a");
      SB();
      if constexpr (T == 3) {
        if constexpr (n + WIN < 128) B[n % WIN] = ldb(k, sb, n + WIN);
        if constexpr (m == 26) bar();
        hook(ic);
      } else {
        constexpr int J = redslice(3 * m + T);
        if constexpr (qd > 0 && J >= 0) red(IC<qd - 1>{}, IC<J>{});
        else slot(IC<freeslot(3 * n + T)>{});
      }
      SB();
    });
  });
  asm volatile("s_nop 7" : "+v"(acc[1][0][0]), "+v"(acc[1][0][1]), "+v"(acc[1][1][0]), "+v"(acc[1][1][1]), "+v"(acc[1][2][0]), "+v"(acc[1][2][1]), "+v"(acc[1][3][0]), "+v"(acc[1][3][1]));
  sfor<9>([&](auto jc) AI { red(IC<3>{}, jc); });
  {
    f4 r[6];
#pragma unroll
    for (int u = 0; u < 6; ++u) r[u] = ldl<f4>(wa, u * 1024);
    f2 xu[4];
#pragma unroll
    for (int j = 0; j < 4; ++j) { xu[j] = unpk(xr[3][j]) * xg; PIN(xu[j]); }
    last();
    f4 a = (acc[1][0][0] + r[0]) + (r[2] + r[4]), b = (acc[1][0][1] + r[1]) + (r[3] + r[5]);
#pragma unroll
    for (int j = 0; j < 4; ++j) { auto s = __builtin_amdgcn_permlane16_swap(fbits(a[j]), fbits(b[j]), false, false); a[j] = bitsf(s[0]); b[j] = bitsf(s[1]); }
    st16c(k.ws, pv, ps + 3 * (L ? 512 : 1024), v4u{san(pk(f2{a[0], a[1]} + xu[0])), san(pk(f2{a[2], a[3]} + xu[1])), san(pk(f2{b[0], b[1]} + xu[2])), san(pk(f2{b[2], b[3]} + xu[3]))});
  }
}

DI v4u poll16(rsrc_t r, u32 v, u32 s) { v4u b; SPIN { b = ld16c(r, v, s); asm volatile("" ::: "memory"); if (!__any(max4(b) == SENT)) break; } return b; }
DI u32 poll4(rsrc_t r, u32 v, u32 s) { u32 b; SPIN { b = ld4c(r, v, s); asm volatile("" ::: "memory"); if (!__any(b == SENT)) break; } return b; }
DI void pollflag(rsrc_t r, u32 v, u32 s, v2u f, v2u tag) {
  if (__builtin_expect(!__all(f[0] == tag[0] && f[1] == tag[1]), 0)) { SPIN { f = ld8c(r, v, s); asm volatile("" ::: "memory"); if (__all(f[0] == tag[0] && f[1] == tag[1])) break; } }
}

struct Ffn { v4u G[4]; u32 D[8]; v4u zf[4]; f4 gu; float p[8], g, u, y0, y1; u32 h, a; };
constexpr int NG = 16, NP = 24;
template <int J> DI void gemv1_slice(const Ctx& k, Ffn& f, u32 z) {
  if constexpr (J == 0) { f.a = opq(L_ZS + k.tid * 4); stl<u32>(f.a, 0, z); }
  if constexpr (J == 1) { f.a = opq(L_ZS + k.w * 256 + (k.lane >> 4) * 16); }
  if constexpr (J == 2) { f.zf[0] = ldl<v4u>(f.a); f.zf[1] = ldl<v4u>(f.a, 64); }
  if constexpr (J == 3) { f.zf[2] = ldl<v4u>(f.a, 128); f.zf[3] = ldl<v4u>(f.a, 192); }
  if constexpr (J == 8) { MFMA0(f.gu, f.G[0], f.zf[0], "v", "v"); MFMA(f.gu, f.G[1], f.zf[1], "v", "v"); MFMA(f.gu, f.G[2], f.zf[2], "v", "v"); MFMA(f.gu, f.G[3], f.zf[3], "v", "v"); }
  if constexpr (J == 9) { f.a = opq(L_P1 + k.w * 64 + (k.lane >> 4) * 16); }
  if constexpr (J == 15) { asm volatile("s_nop 3" : "+v"(f.gu)); stl<f4>(f.a, 0, f.gu); }
}
template <int J> DI void push_slice(const Ctx& k, Ffn& f, u32 oy) {
  if constexpr (J == 0) f.a = opq(L_P1 + (k.lane & 7) * 4);
  if constexpr (J >= 1 && J < 5) { f.p[J - 1] = ldl<float>(f.a, (J - 1) * 64); f.p[J + 3] = ldl<float>(f.a, (J - 1) * 64 + 32); }
  if constexpr (J == 7) { f.g = f.p[0] + f.p[1]; f.u = f.p[4] + f.p[5]; PIN2(f.g, f.u); }
  if constexpr (J == 8) { f.p[2] += f.p[3]; f.p[6] += f.p[7]; PIN2(f.p[2], f.p[6]); }
  if constexpr (J == 9) { f.g += f.p[2]; f.u += f.p[6]; PIN2(f.g, f.u); }
  if constexpr (J == 10) { f.h = pk(f2{f.g, f.u}); PIN(f.h); }
  if constexpr (J == 11) { f.g = blo(f.h); f.u = bhi(f.h); PIN2(f.g, f.u); }
  if constexpr (J == 12) { f.y0 = f.g * -1.44269504f; PIN(f.y0); }
  if constexpr (J == 13) { f.y0 = __builtin_amdgcn_exp2f(f.y0); PIN(f.y0); }
  if constexpr (J == 14) { f.y0 += 1.f; PIN(f.y0); }
  if constexpr (J == 15) { f.y0 = __builtin_amdgcn_rcpf(f.y0); PIN(f.y0); }
  if constexpr (J == 16) { f.h = pk(f2{f.y0 * f.g, 0.f}); PIN(f.h); }
  if constexpr (J == 17) { f.g = blo(f.h) * f.u; PIN(f.g); }
  if constexpr (J == 18) { f.u = dppf<0xB1>(f.g); PIN(f.u); }
  if constexpr (J == 19) { f.h = pk(f2{f.g, f.u}); PIN(f.h); }
  if constexpr (J == 21) {
    u32 hp[4];
#pragma unroll
    for (int p = 0; p < 4; ++p) hp[p] = __builtin_amdgcn_readlane(f.h, 2 * p);
    asm volatile("s_nop 1\n\tv_dot2_f32_bf16 %0, %2, %10, 0\n\tv_dot2_f32_bf16 %1, %3, %10, 0\n\tv_dot2_f32_bf16 %0, %4, %11, %0\n\tv_dot2_f32_bf16 %1, %5, %11, %1\n\t"
                 "v_dot2_f32_bf16 %0, %6, %12, %0\n\tv_dot2_f32_bf16 %1, %7, %12, %1\n\tv_dot2_f32_bf16 %0, %8, %13, %0\n\tv_dot2_f32_bf16 %1, %9, %13, %1\n\ts_nop 2"
                 : "=&v"(f.y0), "=&v"(f.y1)
                 : "v"(f.D[0]), "v"(f.D[1]), "v"(f.D[2]), "v"(f.D[3]), "v"(f.D[4]), "v"(f.D[5]), "v"(f.D[6]), "v"(f.D[7]), "s"(hp[0]), "s"(hp[1]), "s"(hp[2]), "s"(hp[3]));
  }
  if constexpr (J == 20) { const u32 ln = opq(k.lane); f.a = (ln >> 2) * 7168 + (ln & 3) * 8; PIN(f.a); }
  if constexpr (J == 22) { f.y0 = bitsf(san(fbits(f.y0))); f.y1 = bitsf(san(fbits(f.y1))); PIN2(f.y0, f.y1); }
  if constexpr (J == 23) st8c(k.ws, f.a, oy, v2u{fbits(f.y0), fbits(f.y1)});
}
DI void yred_load(const Ctx& k, u32 oy, v4u (&Y)[2]) { const u32 v = opq(k.tid * 16); Y[0] = ld16c(k.ws, v, oy); Y[1] = ld16c(k.ws, v, oy + 4096); }
DI bool yred_bad(const Ctx& k, const v4u (&Y)[2]) { return __any(max4(Y[0]) == SENT || (k.tid < 192 && max4(Y[1]) == SENT)); }
DI void yred_poll(const Ctx& k, u32 oy, v4u (&Y)[2]) { SPIN { yred_load(k, oy, Y); asm volatile("" ::: "memory"); if (!yred_bad(k, Y)) break; } }
DI void yred_part(const Ctx& k, const v4u (&Y)[2]) {
  float v[4];
#pragma unroll
  for (int j = 0; j < 4; ++j) { v[j] = bitsf(Y[0][j]) + (k.tid < 192 ? bitsf(Y[1][j]) : 0.f); PIN(v[j]); }
  auto s0 = __builtin_amdgcn_permlane32_swap(fbits(v[0]), fbits(v[2]), false, false);
  auto s1 = __builtin_amdgcn_permlane32_swap(fbits(v[1]), fbits(v[3]), false, false);
  auto s2 = __builtin_amdgcn_permlane16_swap(fbits(bitsf(s0[0]) + bitsf(s0[1])), fbits(bitsf(s1[0]) + bitsf(s1[1])), false, false);
  float t = bitsf(s2[0]) + bitsf(s2[1]);
  t += dppf<0x128>(t); t += dppf<0x124>(t); t += dppf<0x122>(t);
  const u32 ln = opq(k.lane);
  stl<float>(L_P2 + k.w * 32 + (ln & 1) * 16 + (ln >> 4) * 4, 0, t);
}
DI float sigm(float r) { return rbf(__builtin_amdgcn_rcpf(1.f + __expf(-rbf(r)))); }
DI f2 yred_fin(u32 p, float sg) {
  const u32 pa = opq(L_P2 + p * 8);
  const f2 y = (ldl<f2>(pa) + ldl<f2>(pa, 32)) + (ldl<f2>(pa, 64) + ldl<f2>(pa, 96));
  return unpk(pk(unpk(pk(y)) * sg));
}
constexpr int ldag(int n) { const int a = n < O0 ? OW0 : OW0 + (n - O0) / OS + 1 < OWN ? OW0 + (n - O0) / OS + 1 : OWN, b = n >= 64 - (NPRE - OWN) ? n + 1 - (64 - (NPRE - OWN)) : 0; return a + b; }
constexpr int wx(int s) { return s <= 4 ? 0 : (s - 4) * WX / 12; }
constexpr int wg1(int n) { const int v = WX + (n + 1) * WG; return v < 32 ? v : 32; }
constexpr int wstep(int p) { int n = -1; while (wg1(n) <= p) ++n; return n; }
constexpr int wxstep(int p) { int s = 0; while (s < 16 && wx(s + 1) <= p) ++s; return s; }
constexpr int pfx(int s) { const int v = s <= PX0 ? 0 : (s - PX0 - 1) / PXS + 1; return v < PXN ? v : PXN; }
constexpr int pfg(int n) { const int v = n < PF0 ? 0 : PFB + (n - PF0) / PFS; return v < 8 - PXN - PFE ? v : 8 - PXN - PFE; }
constexpr int patg(int n) { return NOINIT || n < PAT0 ? 0 : (n - PAT0) / PATS + 1 < 5 ? (n - PAT0) / PATS + 1 : 5; }

__global__ __launch_bounds__(256, 1) void fnet_mod_kernel(const u16* x, const u16* Cs, const u16* Ss, const u16* Cd, const u16* Sd, const u16* wr,
                                                          const u16* wg, const u16* wu, const u16* wd, u16* out, unsigned char* wsp) {
  extern __shared__ __attribute__((aligned(16))) unsigned char smem[];
  STAMP(0);
  Ctx k;
  k.tid = threadIdx.x; k.lane = k.tid & 63; k.w = rfl(k.tid >> 6);
  const u32 b = blockIdx.x, x8 = b & 7, r5 = b >> 3;
  u32 pf0;
  {
    u64 pc; u32 len;
    asm volatile("s_getpc_b64 %0\nfm_code_begin:\n\ts_mov_b32 %1, fm_code_end-fm_code_begin" : "=s"(pc), "=s"(len));
    u32 o = ((k.tid & 12) ? (k.tid >> 4) * 3 + ((k.tid >> 2) & 3) - 1 : (k.tid >> 4) * 32 + r5) * 128; o = o < len ? o : 0;
    asm volatile("global_load_dword %0, %1, %2" : "=v"(pf0) : "v"(o), "s"(pc) : "memory");
  }
  SB();
  k.c = (x8 & 1) * 4 + (r5 & 3); k.i = (x8 >> 1) * 8 + (r5 >> 2); k.cg = k.c & 3; k.im = k.c >> 2; k.rep = x8; k.rot = ROT == 2 ? 2 * k.cg : ROT ? k.i & 7 : 0;
  k.ws = mkrs(wsp, WS_BYTES);
  k.l16 = opq(k.lane * 16);
  const u32 w = k.w, i = k.i, c = k.c, cg = k.cg, im = k.im, me = i * 8 + c;
  u32 lane = k.lane, tid = k.tid, q = lane >> 4, t16 = lane & 15, sq = ((q & 1) << 1) | (q >> 1);
#define REFRESH() do { tid = opq(tid); lane = tid & 63; q = lane >> 4; t16 = lane & 15; sq = ((q & 1) << 1) | (q >> 1); k.tid = tid; k.lane = lane; } while (0)
  asm volatile("" :: "v"((u32)(size_t)smem));

  u32 pa = 0; v2u ep;
  const rsrc_t rw = mkrs((im ? Sd : Cd) + cg * 65536, 128 * 1024), rx = mkrs(x, 2048 * 1024), rm = mkrs(im ? Ss : Cs, 2048 * 4096);
  const bool vA = r5 < 28, red = b < 64;
  const u32 jA = x8 * 28 + (vA ? r5 : 0);
  STAMPX(2, 1);
  v4u A[64];
  const u32 av = (lane >> 2) * 4096 + (lane & 3) * 16;
  u32 as[4];
#pragma unroll
  for (int T = 0; T < 4; ++T) as[T] = (64 * i + 16 * ((T + w) & 3)) * 4096 + w * 1024;
#define LDA(n) asm volatile("buffer_load_dwordx4 %0, %1, %2, %3 offen offset:%4" : "=a"(A[n]) : "v"(av), "s"(rm), "s"(as[(n) >> 4] + (((((n) & 15) >> 1) + k.rot) & 7) * 128), "i"(((n) & 1) * 64) : "memory")
#define AIDX(u) ((((u) >> 1) & 3) * 16 + ((u) >> 3) * 2 + ((u) & 1))
#pragma unroll
  for (int u = 0; u < OW0; ++u) LDA(AIDX(u));
  v4u XR[16];
  const u32 xv = (64 * i + 16 * w + (lane >> 2)) * 1024 + (lane & 3) * 16;
#define LDX(s) asm volatile("buffer_load_dwordx4 %0, %1, %2, %3 offen offset:%4" : "=v"(XR[s]) : "v"(xv), "s"(rx), "s"(((((s) >> 2) + cg) & 3) * 256), "i"(((s) & 3) * 64) : "memory")
  u32 pw_ = 0;
#define PFWI() asm volatile("buffer_load_dword %0, %1, %2, 0 offen" : "+v"(pw_) : "v"((w * 32 + (i & 7) * 4 + ((lane >> 4) & 3)) * 1024 + ((lane >> 1) & 7) * 128), "s"(rw) : "memory")
  if (PFW == 1) PFWI();
#pragma unroll
  for (int s = 0; s < 12; ++s) { LDX(s); if (PFW == 2 && s == 3) PFWI(); }
  STAMPX(2, 2);
  u32 wv[8];
#pragma unroll
  for (int j = 0; j < 8; ++j) wv[j] = ((((lane >> 4) + cg) & 3) * 256 + (((lane ^ (w * 8)) & 15) << 4)) ^ (j << 4);
#define LDW(n) asm volatile("s_add_u32 m0, %0, %1\n\ts_nop 0\n\tbuffer_load_dwordx4 %2, %3, m0 offen lds" :: "s"(w * 8192), "i"(((n) >> 3) * 32768 + ((n) & 7) * 1024), "v"(wv[(n) & 7]), "s"(rw) : "memory", "scc")
  STAMPX(2, 3);
#define PFA(j) asm volatile("buffer_load_dword %0, %1, %2, %3 offen offset:%4" : "+v"(pa) : "v"(av), "s"(rm), "s"(NOPF ? 0x7f000000u : (64 * i + 16 * cg) * 4096 + w * 1024 + (((j) + k.rot) & 7) * 128), "i"(0) : "memory")
  SB();
  const u32 stw = opq(L_RED + w * 6144 + (lane >> 2) * 64 + (((lane & 3) ^ ((lane >> 5) * 3)) << 4));
  const u32 strd = L_RED + w * 6144 + t16 * 64;
  const u32 gx = (t16 >> 3) * 3;
  const u32 xrd = opq(strd + ((sq ^ gx) << 4));
  v4u Z[16];
  f4 S, Q;
  {
    const v4u ones = {0x3F803F80u, 0x3F803F80u, 0x3F803F80u, 0x3F803F80u};
    sfor<18>([&](auto sc) AI {
      constexpr int s = sc;
      if constexpr (s < 16) {
        asm volatile("s_waitcnt vmcnt(%1)" : "+v"(XR[s]) : "i"((s <= 4 ? 11 : 16 - s + wx(s)) + pfx(s) + (PFW == 2 && s <= 3)) : "memory");
        if (s == 0) STAMPX(2, 4); if (s == 4) STAMPX(2, 5); if (s == 8) STAMPX(2, 6); if (s == 15) STAMPX(2, 7);
        if constexpr (s < 4) LDX(12 + s);
        if constexpr (s == 4) asm volatile("buffer_load_dwordx2 %0, %1, %2, 0 offen sc1" : "=v"(ep) : "v"(0u), "s"(k.ws) : "memory");
        _Pragma("unroll") for (int p = wx(s); p < wx(s + 1); ++p) LDW(p);
        if constexpr (pfx(s + 1) > pfx(s)) PFA(pfx(s));
        stl<v4u>(stw, (s % 6) * 1024, XR[s]);
        Z[s] = ldl<v4u>(xrd, (s % 6) * 1024);
        SB();
      }
      if constexpr (s == 2) { asm volatile("s_nop 1\n\tv_mfma_f32_16x16x32_bf16 %0, %1, %2, 0" : "=&v"(S) : "v"(ones), "v"(Z[0])); MFMA0(Q, Z[0], Z[0], "v", "v"); SB(); }
      if constexpr (s > 2) { MFMA(S, ones, Z[s - 2], "v", "v"); MFMA(Q, Z[s - 2], Z[s - 2], "v", "v"); SB(); }
    });
  }
  STAMP(1); STAMPX(2, 8);
  float mean, var, rstd;
  asm volatile("s_nop 7" : "+v"(S), "+v"(Q));
  stats(lane, S, Q, mean, var);
  rstd = __builtin_amdgcn_rsqf(var + 1e-5f);
  { const u32 a = opq(L_ST + w * 128 + t16 * 4); stl<float>(a, 0, rstd); stl<float>(a, 64, -mean * rstd); }
  static_assert(PX0 >= 3 && pfx(16) == PXN, "schedule");
  VM(WX - 8 + pfx(16) - pfx(wxstep(7)));
  bar();
  STAMP(2); STAMPX(2, 9);
  gemm1<0>(k, Z, [&](auto ic) AI {
    constexpr int n = ic;
    _Pragma("unroll") for (int p = wg1(n - 1); p < wg1(n); ++p) LDW(p);
    _Pragma("unroll") for (int p = pfg(n - 1); p < pfg(n); ++p) PFA(PXN + p);
    _Pragma("unroll") for (int u = ldag(n - 1); u < ldag(n); ++u) LDA(AIDX(u));
    static_assert(ldag(63) == NPRE, "schedule");
    if constexpr (patg(n) > patg(n - 1)) {
      constexpr int z = patg(n - 1);
      st16c(k.ws, k.l16, z < 4 ? O_PQ0 + (me * 16 + w * 4 + z) * 1024 : w < 2 ? O_SM1 + (me * 2 + w) * 1024 : WS_BYTES, mksent());
    }
    if constexpr (n == 8 || n == 24 || n == 40 || n == 56) {
      constexpr int lastp = n == 8 ? 15 : n == 24 ? 23 : 31, before = wstep(lastp) - 1, lda = ldag(n) - ldag(before);
      static_assert(wstep(15) <= 8 && wstep(23) <= 24 && wstep(31) < 28, "schedule");
      constexpr int young = n == 56 ? 40 : lda + wg1(n) - 1 - lastp + (n == 40 ? 1 : 0) + pfg(n) - pfg(before) + patg(n) - patg(before) + (before < -1 ? pfx(16) - pfx(wxstep(lastp)) : 0);
      STAMPX(2, 10 + n / 16);
      asm volatile("s_waitcnt vmcnt(%0) lgkmcnt(8)\n\ts_barrier" :: "i"(young) : "memory");
    }
  });
  asm volatile("" ::: "memory");
  SB();
  STAMP(3); STAMPX(2, 16);
#define PERM(n, u) asm volatile("ds_write_b128 %1, %0 offset:%3\n\tds_read_b128 %0, %2 offset:%3" : "+a"(A[n]) : "v"(stw), "v"(ard), "i"(((u) % 6) * 1024) : "memory")
  const u32 ard = strd + ((q ^ gx) << 4);
  const u32 flagv = lane < 32 ? (lane * 8 + c) * 512 + (i >> 3) * 128 + (i & 7) * 8 : (i * 8 + (lane & 7)) * 512 + ((lane >> 3) & 3) * 128 + 64 + c * 8;
  u64 epoch; v2u tag, fl;
  v4u B[WIN], wv0;
  const u32 sb0 = O_AB0 + c * 524288 + w * 32768, sb1 = O_AB1 + c * 524288 + w * 32768;
#pragma unroll
  for (int p = 8 - PFE; p < 8; ++p) PFA(p);
  constexpr int UF = NPRE < FLU ? NPRE : FLU, UW = 64 - LAG > WU ? 64 - LAG : WU;
  static_assert(UW < 64 - NJ, "schedule");
  sfor<64 - NJ>([&](auto ic) AI {
    constexpr int u = ic, a = u + LAG;
    if constexpr (a >= NPRE && a < 64) LDA(AIDX(a));
    if constexpr (u == UW - 12) asm volatile("buffer_load_dwordx2 %0, %1, %2, %3 offen sc1" : "=v"(fl) : "v"((lane & 15) * 8), "s"(k.ws), "s"(O_FLAG + me * 512 + w * 128) : "memory");
    if constexpr (u == UW) {
      STAMP(5); STAMPX(2, 18);
      constexpr int last = UW - 12 + LAG;
      asm volatile("s_waitcnt vmcnt(%2)" : "+v"(fl), "+v"(pa) : "i"(last < 63 ? 63 - last : 0) : "memory");
      pollflag(k.ws, (lane & 15) * 8, O_FLAG + me * 512 + w * 128, fl, tag);
      STAMP(6); STAMPX(2, 19);
    }
    if constexpr (u >= UW) B[u - UW] = ldb(k, sb0, u - UW);
    constexpr int top = a < NPRE - 1 ? NPRE - 1 : a < 63 ? a : 63;
    constexpr int yg = top - u + (u < NPRE ? 1 + PFE : 0) + (u >= UW ? u - UW + 1 : 0);
    if constexpr (u != UF) VM(yg < 63 ? yg : 63);
    if constexpr (u == UF) {
      asm volatile("s_waitcnt vmcnt(%4)\n\ts_barrier" : "+v"(ep), "+v"(pf0), "+v"(pa), "+v"(pw_) : "i"(top - NPRE + 1 + PFE < yg ? top - NPRE + 1 + PFE : yg) : "memory");
      STAMP(4); STAMPX(2, 17);
      epoch = ((u64)rfl(ep[1]) << 32) | rfl(ep[0]);
      const u64 tag64 = (epoch + 0x632BE59BD9B4E019ull) * 0x9E3779B97F4A7C15ull;
      tag = v2u{(u32)tag64, (u32)(tag64 >> 32)};
      if (w == 0) st8c(k.ws, flagv, O_FLAG, tag);
    }
    PERM(AIDX(u), u);
    if constexpr ((u & 3) == 3) asm volatile("s_waitcnt lgkmcnt(6)" ::: "memory");
    if constexpr (u == 31) STAMPX(2, 21);
    SB();
  });
#pragma unroll
  for (int u = 64 - NJ - UW; u < WIN; ++u) { B[u] = ldb(k, sb0, u); SB(); }
  v4u GS[4];
  {
    const u32 v = (lane & 31) < 28 ? (lane >> 5) * 3584 + (lane & 31) * 16 : 0x7fffff00u;
    const rsrc_t rgu = mkrs((w & 1) ? wu : wg, 512 * 3584);
#pragma unroll
    for (int r = 0; r < 4; ++r) GS[r] = __builtin_amdgcn_raw_buffer_load_b128(rgu, v, (r5 * 16 + (w >> 1) * 8 + 2 * r) * 3584 + x8 * 448, 0);
  }
  wv0 = ld16(mkrs(wr, 1024), k.l16, 0);
  asm volatile("s_waitcnt lgkmcnt(0)" ::: "memory");
  SB();
  float sumw, sx;
  STAMP(7); STAMPX(2, 20);

  REFRESH();
  v4u PQ[16][2]; u32 Dw[8];
  const u32 pqs = O_PQ0 + (i * 4 + w) * 32768;
  auto ldpq = [&](int s, int m) AI { return ld16c(k.ws, k.l16 + (s & 3) * 1024, pqs + m * 16384 + (((s >> 2) + cg) & 3) * 4096); };
  gemm2<0>(k, A, B, sb0, Z, im ? 0.f : 1.f, 0.f, [&](auto ic) AI {
    constexpr int n = ic;
    if constexpr (n == 0) { STAMPX(3, 1); STAMPC(22); } if constexpr (n == 110) STAMPC(23); if constexpr (n == 15) STAMPX(3, 2); if constexpr (n == 31) STAMPX(3, 5);
    if constexpr (n == 47) STAMPX(3, 8); if constexpr (n == 63) STAMPX(3, 9); if constexpr (n == 79) STAMPX(3, 10);
    if constexpr (n == 95) STAMPX(3, 11); if constexpr (n == 110) STAMPX(3, 12); if constexpr (n == 127) STAMPX(3, 13);
    if constexpr (n < NJ) asm volatile("ds_write_b128 %1, %0 offset:%3\n\tds_read_b128 %0, %2 offset:%3" : "+a"(A[AIDX(64 - NJ + n)]) : "v"(stw), "v"(ard), "i"((n % 6) * 1024) : "memory");
    if constexpr (n == NJ) asm volatile("s_waitcnt lgkmcnt(0)" ::: "memory");
    if constexpr (n == 19) bar();
    if constexpr (n >= 20 && n < 24) stl<v4u>(opq(L_RED + (lane & 31) * 528 + (lane >> 5) * 32 + (w >> 1) * 256 + (w & 1) * 16), (n - 20) * 64, GS[n - 20]);
    if constexpr (n == 27) {
      const u32 a = opq(L_RED + (w * 8 + (lane >> 5)) * 528 + (lane & 31) * 16);
#pragma unroll
      for (int u = 0; u < 4; ++u) GS[u] = ldl<v4u>(a, u * 1056);
    }
    if constexpr (n == 32) bar();
    if constexpr (n == 111) {
      const rsrc_t rd = mkrs(wd, 1792 * 1024);
      const u32 vd = opq(tid * 4);
#pragma unroll
      for (int j = 0; j < 8; ++j) Dw[j] = ld4(rd, vd + (j & 3) * 1024, jA * 8192 + (j >> 2) * 4096);
    }
    if constexpr ((n == 56 || n == 66 || n == 88 || n == 98) && !NOINIT)
      st16c(k.ws, (lane >> 5) * 256 + (q & 1) * 1024 + t16 * 16, O_AB1 + c * 524288 + (2 * i + (w >> 1)) * 2048 + (w & 1) * 512 + (n == 56 ? 0 : n == 66 ? 1 : n == 88 ? 2 : 3) * 131072, mksent());
    if constexpr (n >= 128 - NPQ) { constexpr int u = n - (128 - NPQ), s = ((u >> 1) & 3) * 4 + (u >> 3); PQ[s][u & 1] = ldpq(s, u & 1); }
  }, [&](auto jc) AI {
    constexpr int J = jc - 60;
    if constexpr (J == 0) stl<v4u>(opq(L_OW + ((k.l16 - cg * 256) & 1023)), 0, wv0);
    if constexpr (J == 3) stl<v4u>(opq(L_OW + 1024 + k.l16), 0, v4u{0x3F803F80u, 0x3F803F80u, 0x3F803F80u, 0x3F803F80u});
    if constexpr (J == 5) { sumw = blo(wv0[0]); sx = bhi(wv0[0]); PIN2(sumw, sx); }
    if constexpr (J >= 6 && J < 12) { constexpr int j = (J - 6) / 2 + 1; if constexpr (J & 1) { sx += bhi(wv0[j]); PIN(sx); } else { sumw += blo(wv0[j]); PIN(sumw); } }
    if constexpr (J == 12) { sumw += sx; PIN(sumw); }
    if constexpr (J >= 13 && J < 17) { sumw += dppf<0x120 + (8 >> (J - 13))>(sumw); PIN(sumw); }
    if constexpr (J == 17) { sumw = xsum16(sumw); PIN(sumw); }
    if constexpr (J == 19) { sumw = xsum32(sumw); PIN(sumw); }
    if constexpr (J == 21) sumw = bitsf(rfl(fbits(sumw)));
  }, [&]() AI {});
  STAMP(8); STAMPX(3, 14); STAMPX(6, 1);
  REFRESH();
  const u32 owa = opq(L_OW + ((t16 & 3) == 1 ? 0 : 1024) + sq * 16);
  v4u ow[2];
  auto take = [&](int s, int prev, int p) AI {
    if (prev == 0) { asm volatile("s_nop 1\n\tv_mfma_f32_16x16x32_bf16 %0, %1, %2, 0" : "=&v"(S) : "v"(ow[p ^ 1]), "v"(Z[0])); MFMA0(Q, Z[0], Z[0], "v", "v"); }
    else if (prev > 0) { asm volatile("s_nop 1\n\tv_mfma_f32_16x16x32_bf16 %0, %1, %2, %0" : "+v"(S) : "v"(ow[p ^ 1]), "v"(Z[prev])); MFMA(Q, Z[prev], Z[prev], "v", "v"); }
    SB();
    if (s >= 0) {
      ow[p] = ldl<v4u>(owa, s * 64);
      f2 a[4], bq[4];
#pragma unroll
      for (int j = 0; j < 4; ++j) { a[j] = unpk(PQ[s][0][j]); bq[j] = unpk(PQ[s][1][j]); PIN2(a[j], bq[j]); }
      SB();
#pragma unroll
      for (int j = 0; j < 4; ++j) Z[s][j] = pk(a[j] - bq[j]);
      PIN(Z[s]);
      SB();
    }
  };
  auto ldq = [&](int r) AI {
#pragma unroll
    for (int s = r; s < 16; s += 4) { PQ[s][0] = ldpq(s, 0); PQ[s][1] = ldpq(s, 1); }
  };
  auto badq = [&](int r) AI {
    u32 mx = 0;
#pragma unroll
    for (int s = r; s < 16; s += 4) mx = umax(mx, umax(max4(PQ[s][0]), max4(PQ[s][1])));
    return __any(mx == SENT);
  };
  auto pollq = [&](int r) AI { SPIN { ldq(r); asm volatile("" ::: "memory"); if (!badq(r)) break; } };
  auto takeq = [&](int r) AI {
#pragma unroll
    for (int u = 0; u < 4; ++u) take(4 * u + r, u ? 4 * u - 4 + r : r ? 11 + r : -1, u & 1);
  };
  auto takeq2 = [&](int r, auto&& side) AI {
#pragma unroll
    for (int u = 0; u < 4; ++u) { take(4 * u + r, u ? 4 * u - 4 + r : r ? 11 + r : -1, u & 1); side(u); SB(); }
  };
#pragma unroll
  for (int u = NPQ; u < 16; ++u) { const int s = ((u >> 1) & 3) * 4 + (u >> 3); PQ[s][u & 1] = ldpq(s, u & 1); }
  SB();
  if (__builtin_expect(badq(0), 0)) WAITT(5, pollq(0));
  takeq2(0, [&](int u) AI { PQ[4 * u + 2][0] = ldpq(4 * u + 2, 0); PQ[4 * u + 2][1] = ldpq(4 * u + 2, 1); });
  Ffn f;
  v4u GU[4];
  SB();
  if (__builtin_expect(badq(1), 0)) WAITT(6, pollq(1));
  const u32 j2 = w * 8 + (lane >> 5), gtv = j2 * 16384 + (lane & 31) * 16;
  takeq2(1, [&](int u) AI { st16c(k.ws, j2 + 2 * u < 28 ? gtv : 0x7fffff00u, O_GT + (x8 * 32 + 2 * u) * 16384 + r5 * 512, GS[u]); });
  STAMP(9); STAMPX(3, 15); STAMPX(6, 2);
  ldq(3);
  SB();
  if (__builtin_expect(badq(2), 0)) WAITT(7, pollq(2));
  takeq(2);
#pragma unroll
  for (int p = 0; p < 4; ++p) { f.D[2 * p] = __builtin_amdgcn_perm(Dw[2 * p + 1], Dw[2 * p], 0x05040100u); f.D[2 * p + 1] = __builtin_amdgcn_perm(Dw[2 * p + 1], Dw[2 * p], 0x07060302u); PIN2(f.D[2 * p], f.D[2 * p + 1]); }
  SB();
  if (__builtin_expect(badq(3), 0)) WAITT(8, pollq(3));
  asm volatile("s_barrier" ::: "memory");
  if (w == 0) st8c(k.ws, lane < 32 ? O_FLAG2 + (lane * 8 + c) * 512 + (i >> 3) * 128 + (i & 7) * 8 : O_FLAG4 + (x8 * 32 + (lane & 31)) * 256 + r5 * 8, 0, tag);
  STAMP(10); STAMPX(3, 16); STAMPX(6, 3);
  takeq(3);
  take(-1, 15, 0);
  asm volatile("s_nop 7" : "+v"(S), "+v"(Q));
  stats(lane, S, Q, mean, var);
  rstd = __builtin_amdgcn_rsqf(var + 1e-5f);
  const float g1 = 1.f + rstd, s3 = g1 * __builtin_amdgcn_rsqf(var * g1 * g1 + 1e-5f);
  { const u32 a = opq(L_ST + w * 128 + t16 * 4); stl<float>(a, 0, s3); stl<float>(a, 64, -mean * s3); }
  if (me == 0 && tid == 0) st8c(k.ws, 0, O_EPOCH, v2u{(u32)(epoch + 1), (u32)((epoch + 1) >> 32)});
  if (!(c & 1) && w == (c >> 1)) {
    const float r = (S[1] - mean * sumw) * rstd;
    const u32 key = mkkey(r, 64 * i + 16 * w + t16), best = rowmax(key);
    const int src = __builtin_ctzll(__ballot(key == best));
    const v4u rec = {best, san(__builtin_amdgcn_readlane(fbits(r), src)), san(__builtin_amdgcn_readlane(fbits(mean), src)), san(__builtin_amdgcn_readlane(fbits(rstd), src))};
    if (lane < 8) st16c(k.ws, lane * 2048, O_KEY1 + (i * 4 + w) * 16, rec);
  }
  float xg = im ? 0.f : g1, xo = im ? 0.f : -mean * rstd;
  PIN2(xg, xo);
  STAMP(11); STAMPX(3, 17); STAMPX(6, 4);
  REFRESH();
  u32 gate, zp, zq, idx1, rso, fv; float m1, rs1, sg1;
  v4u K0, K1, YP[2], R0, YY; v2u h[8];
  const u32 gv = (lane >> 2) * 2048 + (lane & 3) * 512 + 496;
  gemm1<1>(k, Z, [&](auto ic) AI {
    constexpr int n = ic;
    if constexpr (n == 15) STAMPX(3, 18);
    if constexpr (n % 8 == 7) STAMPX(6, 5 + n / 8);
    if constexpr ((n == PT1 || n == PT1 + PTS) && !NOINIT)
      st16c(k.ws, (t16 >> 3) * 16384 + sq * 128 + (t16 & 7) * 16, O_PQ1 + (i * 8 + 2 * w) * 16384 + im * 8192 + cg * 2048 + (n == PT1 ? 2 : 3) * 512, mksent());
    if constexpr (n == PT1 + 2 * PTS && !NOINIT) st16c(k.ws, k.l16, O_SM2 + (me * 4 + w) * 1024, mksent());
    if constexpr (n == FB) { fv = lane < 32 ? O_FLAG4 + (x8 * 32 + r5) * 256 + lane * 8 : O_FLAG2 + me * 512 + w * 128 + (lane & 7) * 8; fl = ld8c(k.ws, fv, 0); }
    if constexpr (n == FC) {
      WAITT(15, pollflag(k.ws, fv, 0, fl, tag));
#pragma unroll
      for (int u = 0; u < 4; ++u) GU[u] = ld16c(k.ws, k.l16 + u * 1024, O_GT + (x8 * 32 + (vA ? r5 : 0)) * 16384 + w * 4096);
    }
    if constexpr (n == KLN) { K0 = ld16c(k.ws, k.l16, O_KEY1 + k.rep * 2048); K1 = ld16c(k.ws, k.l16 + 1024, O_KEY1 + k.rep * 2048); }
    if constexpr (n == GLN) gate = ld4c(k.ws, gv, sb1);
    if constexpr (n == KB - 7) STAMPX(1, 23);
    if constexpr (n == KB - 6) { if (__builtin_expect(__any(umax(max4(K0), max4(K1)) == SENT), 0)) WAITT(9, SPIN { __builtin_amdgcn_s_sleep(KSL); K0 = ld16c(k.ws, k.l16, O_KEY1 + k.rep * 2048); K1 = ld16c(k.ws, k.l16 + 1024, O_KEY1 + k.rep * 2048); asm volatile("" ::: "memory"); if (!__any(umax(max4(K0), max4(K1)) == SENT)) break; }); }
    if constexpr (n == KB - 5) { K0 = K0[0] > K1[0] ? K0 : K1; zp = umax(K0[0], dppu<0x128>(K0[0])); PIN2(K0, zp); }
    if constexpr (n == KB - 4) { zp = umax(zp, dppu<0x124>(zp)); PIN(zp); asm volatile("s_nop 0"); zp = umax(zp, dppu<0x122>(zp)); PIN(zp); }
    if constexpr (n == KB - 3) { zp = umax(zp, dppu<0x121>(zp)); PIN(zp); }
    if constexpr (n == KB - 2) { auto s = __builtin_amdgcn_permlane16_swap(zp, zp, false, false); zp = umax(s[0], s[1]); PIN(zp); }
    if constexpr (n == KB - 1) { auto s = __builtin_amdgcn_permlane32_swap(zp, zp, false, false); zp = umax(s[0], s[1]); PIN(zp); }
    if constexpr (n == KB) {
      STAMPX(3, 19);
      const int src = __builtin_ctzll(__ballot(K0[0] == zp));
      idx1 = 2047u - (rfl(zp) & 2047u);
      rso = O_PQ0 + (idx1 >> 4) * 32768 + (idx1 & 15) * 16;
      sg1 = bitsf(__builtin_amdgcn_readlane(K0[1], src)); m1 = bitsf(__builtin_amdgcn_readlane(K0[2], src)); rs1 = bitsf(__builtin_amdgcn_readlane(K0[3], src));
      const u32 tn = opq(tid), ch = (tn >> 2) & 3, v = (tn >> 4) * 1024 + (((ch & 1) << 1) | (ch >> 1)) * 256 + (tn & 3) * 4;
      zp = ld4c(k.ws, v, rso); zq = ld4c(k.ws, v, rso + 16384);
      STAMPX(1, 21);
      if (__builtin_expect(__any(gate == SENT), 0)) WAITT(10, poll4(k.ws, gv, sb1));
      STAMPX(1, 22);
#pragma unroll
      for (int u = 0; u <= KB - 48; ++u) { B[u] = ldb(k, sb1, u); SB(); }
    }
    if constexpr (n > KB) B[n - 48] = ldb(k, sb1, n - 48);
  });
#pragma unroll
  for (int u = 16; u < WIN; ++u) { B[u] = ldb(k, sb1, u); SB(); }
  STAMP(12); STAMPX(3, 20); STAMPX(6, 13);
  REFRESH();
  v4u P5[2][2], DL[2][2][2], WV[2], wrow; u32 cs = 0, ss = 0;
  float xt[8], wf[8], e0, e1, e2, e3, a0, a1, a2, a3, ga, s3o, pw1;
  const u32 p5s = O_PQ1 + me * 16384 + w * 2048, dso = O_DEL + k.rep * 4096 + w * 512;
  auto lddl = [&]() AI {
    const u32 dv = opq((lane >> 3) * 32);
#pragma unroll
    for (int n = 0; n < 2; ++n)
#pragma unroll
      for (int m = 0; m < 2; ++m)
#pragma unroll
        for (int h = 0; h < 2; ++h) DL[n][m][h] = ld16c(k.ws, dv + n * 256 + h * 16 + m * 2048, dso);
  };
  auto baddl = [&]() AI {
    u32 mx = 0;
#pragma unroll
    for (int n = 0; n < 2; ++n)
#pragma unroll
      for (int m = 0; m < 2; ++m) mx = umax(mx, umax(max4(DL[n][m][0]), max4(DL[n][m][1])));
    return __any(mx == SENT);
  };
  constexpr int G0 = 0, P0 = 30, R_0 = 72, YA = 37, YB = 76, S0 = freeslot(3 * (YB + 11)), DN = 110;
  gemm2<1>(k, A, B, sb1, Z, xg, xo, [&](auto ic) AI {
    constexpr int n = ic;
    if constexpr (n == 0) STAMPX(6, 14); if constexpr (n == 8) STAMPX(6, 15); if constexpr (n == 16) STAMPX(6, 16); if constexpr (n == 24) STAMPX(6, 17); if constexpr (n == 15) STAMPX(4, 1); if constexpr (n == 31) STAMPX(4, 2); if constexpr (n == 47) STAMPX(4, 3); if constexpr (n == 63) STAMPX(4, 4);
    if constexpr (n == 79) STAMPX(4, 5); if constexpr (n == 95) STAMPX(4, 6); if constexpr (n == 111) STAMPX(4, 7); if constexpr (n == 127) STAMPX(4, 8);
    if constexpr (n < 2) { const u32 a = opq(L_RED + w * 6144 + k.l16); stl<v4u>(a, 2 * n * 1024, GU[2 * n]); stl<v4u>(a, (2 * n + 1) * 1024, GU[2 * n + 1]); }
    if constexpr (n == 2) {
      const u32 ta = opq(L_RED + w * 6144 + (8 * q + (t16 >> 2)) * 32 + (t16 & 3) * 8);
      asm volatile("ds_read_b64_tr_b16 %0, %8\n\tds_read_b64_tr_b16 %1, %8 offset:128\n\tds_read_b64_tr_b16 %2, %8 offset:1024\n\tds_read_b64_tr_b16 %3, %8 offset:1152\n\t"
                   "ds_read_b64_tr_b16 %4, %8 offset:2048\n\tds_read_b64_tr_b16 %5, %8 offset:2176\n\tds_read_b64_tr_b16 %6, %8 offset:3072\n\tds_read_b64_tr_b16 %7, %8 offset:3200"
                   : "=&v"(h[0]), "=&v"(h[1]), "=&v"(h[2]), "=&v"(h[3]), "=&v"(h[4]), "=&v"(h[5]), "=&v"(h[6]), "=&v"(h[7]) : "v"(ta) : "memory");
    }
    if constexpr (n == 4) {
      asm volatile("s_waitcnt lgkmcnt(0)" : "+v"(h[0]), "+v"(h[1]), "+v"(h[2]), "+v"(h[3]), "+v"(h[4]), "+v"(h[5]), "+v"(h[6]), "+v"(h[7]) :: "memory");
#pragma unroll
      for (int s = 0; s < 4; ++s) { f.G[s] = v4u{h[2 * s][0], h[2 * s][1], h[2 * s + 1][0], h[2 * s + 1][1]}; PIN(f.G[s]); }
    }
    if constexpr (n == 9) bar();
    if constexpr (n == YA) { if (red) yred_load(k, O_YP1 + b * 7168, YP); }
    if constexpr (n == YA + 12) { if (red) { if (__builtin_expect(yred_bad(k, YP), 0)) WAITT(11, yred_poll(k, O_YP1 + b * 7168, YP)); yred_part(k, YP); } }
    if constexpr (n == YA + 13) { bar(); if (red && w == 0) { const f2 y = yred_fin(lane & 3, sg1); if (lane < 32) st4c(k.ws, (lane >> 2) * 2048 + (lane & 3) * 4, O_Y1 + b * 16, san(pk(y))); } }
    if constexpr (n == YB) YY = ld16c(k.ws, k.l16, O_Y1 + k.rep * 2048);
    if constexpr (n == 111) {
      const u32 v = (64 * i + 8 * c + (lane & 7)) * 4096;
      cs = __builtin_amdgcn_raw_buffer_load_b16(mkrs(Cs, 2048 * 4096), v, idx1 * 2, 0); ss = __builtin_amdgcn_raw_buffer_load_b16(mkrs(Ss, 2048 * 4096), v, idx1 * 2, 0);
    }
    if constexpr (n == 112) { const rsrc_t rr = mkrs(wr, 1024); const u32 v = opq((lane >> 3) * 16); WV[0] = ld16(rr, v, w * 256); WV[1] = ld16(rr, v + 128, w * 256); }
    if constexpr (n == DN) { if (lane < 8) st4c(k.ws, lane * 4096, O_DEL + (128 * c + 4 * i + w) * 4, san(fbits(a0))); }
  }, [&](auto jc) AI {
    constexpr int J = jc;
    if constexpr (J == G0) {
      if (__builtin_expect(__any(umax(zp, zq) == SENT), 0)) {
        const u32 ch = (tid >> 2) & 3, v = (tid >> 4) * 1024 + (((ch & 1) << 1) | (ch >> 1)) * 256 + (tid & 3) * 4;
        WAITT(12, zp = poll4(k.ws, v, rso); zq = poll4(k.ws, v, rso + 16384));
      }
    }
    if constexpr (J == G0 + 1) { e0 = blo(zp); e1 = bhi(zp); PIN2(e0, e1); }
    if constexpr (J == G0 + 2) { e2 = blo(zq); e3 = bhi(zq); PIN2(e2, e3); }
    if constexpr (J == G0 + 3) { e0 -= e2; e1 -= e3; PIN2(e0, e1); }
    if constexpr (J == G0 + 4) { zp = pk(f2{e0, e1}); PIN(zp); }
    if constexpr (J == G0 + 5) { stl<u32>(opq(L_YX + tid * 4), 0, zp); e0 = blo(zp); e1 = bhi(zp); PIN2(e0, e1); }
    if constexpr (J == G0 + 6) { e0 -= m1; e1 -= m1; PIN2(e0, e1); }
    if constexpr (J == G0 + 7) { e0 *= rs1; e1 *= rs1; PIN2(e0, e1); }
    if constexpr (J == G0 + 8) { zp = pk(f2{e0, e1}); PIN(zp); }
    if constexpr (J >= G0 + 9 && J < G0 + 9 + NG) gemv1_slice<J - G0 - 9>(k, f, zp);
    if constexpr (J >= P0 && J < P0 + NP) push_slice<J - P0>(k, f, vA ? O_YP1 + w * 16 * 7168 + jA * 32 : WS_BYTES);
    if constexpr (J == P0 + NP) { sg1 = sigm(sg1); PIN(sg1); }
    if constexpr (J == P0 + NP + 2) { ga = 1.f + rs1; s3o = rs1 * rs1; PIN2(ga, s3o); }
    if constexpr (J == P0 + NP + 3) { s3o = __builtin_amdgcn_rcpf(s3o); PIN(s3o); }
    if constexpr (J == P0 + NP + 4) { s3o = fmaxf(s3o - 1e-5f, 0.f); e3 = ga * ga; PIN2(s3o, e3); }
    if constexpr (J == P0 + NP + 5) { s3o = __builtin_fmaf(s3o, e3, 1e-5f); PIN(s3o); }
    if constexpr (J == P0 + NP + 6) { s3o = __builtin_amdgcn_rsqf(s3o); PIN(s3o); }
    if constexpr (J == P0 + NP + 7) { s3o *= ga; e3 = -m1 * rs1; PIN2(s3o, e3); }
    if constexpr (J == P0 + NP + 8) { const u32 ln = opq(lane), e = 4 * i + w; YY[0] = L_W + e * 1024 + ((((ln >> 4) - cg) & 3) * 16 + ((ln ^ e) & 15)) * 16; PIN(YY[0]); }
    if constexpr (J == P0 + NP + 9) { wrow = ldl<v4u>(YY[0]); pw1 = ldl<float>(opq(L_W1S + (4 * i + w) * 4)); }
    if constexpr (J == R_0 - 4) { R0 = ldl<v4u>(opq(k.l16 + L_YX)); a3 = 0.f; }
    if constexpr (J >= R_0 && J < R_0 + 16) {
      constexpr int j = (J - R_0) / 4, e = (J - R_0) % 4;
      if constexpr (e == 0) { wf[2 * j] = blo(wrow[j]); wf[2 * j + 1] = bhi(wrow[j]); PIN2(wf[2 * j], wf[2 * j + 1]); }
      if constexpr (e == 1) { e0 = blo(R0[j]); e1 = bhi(R0[j]); PIN2(e0, e1); }
      if constexpr (e == 2) { xt[2 * j] = __builtin_fmaf(e0, ga, e3); xt[2 * j + 1] = __builtin_fmaf(e1, ga, e3); PIN2(xt[2 * j], xt[2 * j + 1]); }
      if constexpr (e == 3) { a3 = __builtin_fmaf(e0, wf[2 * j], a3); PIN(a3); a3 = __builtin_fmaf(e1, wf[2 * j + 1], a3); PIN(a3); }
    }
    if constexpr (J == S0 - 1) { gate = umax(umax(YY[0], YY[1]), YY[2]); PIN(gate); }
    if constexpr (J == S0) {
      if (__builtin_expect(__any(umax(gate, YY[3]) == SENT), 0)) WAITT(13, YY = poll16(k.ws, k.l16, O_Y1 + k.rep * 2048));
      a0 = 0.f; a1 = 0.f; a2 = 0.f;
    }
    if constexpr (J > S0 && J <= S0 + 20) {
      constexpr int j = (J - S0 - 1) / 5, e = (J - S0 - 1) % 5;
      if constexpr (e == 0) { e0 = blo(YY[j]); e1 = bhi(YY[j]); PIN2(e0, e1); }
      if constexpr (e == 1) { e0 += xt[2 * j]; e1 += xt[2 * j + 1]; PIN2(e0, e1); }
      if constexpr (e == 2) { a0 += e0; a1 = __builtin_fmaf(e0, e0, a1); PIN2(a0, a1); }
      if constexpr (e == 3) { a2 = __builtin_fmaf(e0, wf[2 * j], a2); a0 += e1; PIN2(a2, a0); }
      if constexpr (e == 4) { a1 = __builtin_fmaf(e1, e1, a1); a2 = __builtin_fmaf(e1, wf[2 * j + 1], a2); PIN2(a1, a2); }
    }
    if constexpr (J == S0 + 21) stl<v4u>(opq(k.l16 + L_YX), 0, YY);
    if constexpr (J >= S0 + 22 && J < S0 + 30) {
      constexpr int r = (J - S0 - 22) / 2;
      if constexpr (!(J - S0 & 1)) { a0 += dppf<0x120 + (8 >> r)>(a0); a1 += dppf<0x120 + (8 >> r)>(a1); PIN2(a0, a1); }
      else { a2 += dppf<0x120 + (8 >> r)>(a2); a3 += dppf<0x120 + (8 >> r)>(a3); PIN2(a2, a3); }
    }
    if constexpr (J == S0 + 30) { auto s = __builtin_amdgcn_permlane16_swap(fbits(a0), fbits(a1), false, false); e0 = bitsf(s[0]); e1 = bitsf(s[1]); PIN2(e0, e1); }
    if constexpr (J == S0 + 31) { auto s = __builtin_amdgcn_permlane16_swap(fbits(a2), fbits(a3), false, false); a2 = bitsf(s[0]); a3 = bitsf(s[1]); PIN2(a2, a3); }
    if constexpr (J == S0 + 32) { e0 += e1; a2 += a3; PIN2(e0, a2); }
    if constexpr (J == S0 + 33) { auto s = __builtin_amdgcn_permlane32_swap(fbits(e0), fbits(a2), false, false); e0 = bitsf(s[0]); a2 = bitsf(s[1]); PIN2(e0, a2); }
    if constexpr (J == S0 + 34) { e0 += a2; PIN(e0); }
    if constexpr (J == S0 + 35) { a0 = bitsf(__builtin_amdgcn_readlane(fbits(e0), 0)) * (1.f / 512); a1 = bitsf(__builtin_amdgcn_readlane(fbits(e0), 16)) * (1.f / 512); PIN2(a0, a1); }
    if constexpr (J == S0 + 36) { a2 = bitsf(__builtin_amdgcn_readlane(fbits(e0), 32)); a3 = bitsf(__builtin_amdgcn_readlane(fbits(e0), 48)); PIN2(a2, a3); }
    if constexpr (J == S0 + 37) { a1 = __builtin_fmaf(-a0, a0, a1); a2 = __builtin_fmaf(-a0, pw1, a2); PIN2(a1, a2); }
    if constexpr (J == S0 + 38) { a3 = __builtin_fmaf(-m1, pw1, a3); a1 = fmaxf(a1, 0.f); PIN2(a3, a1); }
    if constexpr (J == S0 + 39) { a3 *= s3o; a1 += 1e-5f; PIN2(a3, a1); }
    if constexpr (J == S0 + 40) { a1 = __builtin_amdgcn_rsqf(a1); PIN(a1); }
    if constexpr (J == S0 + 41) { a0 = __builtin_fmaf(a1, a2, -a3); PIN(a0); }
    static_assert(S0 + 41 < freeslot(3 * DN + 3), "the correction is ready before it is stored");
  }, [&]() AI { lddl(); SB(); });
  STAMP(13); STAMPX(4, 9);
  REFRESH();
  if (__builtin_expect(baddl(), 0)) WAITT(14, SPIN { lddl(); asm volatile("" ::: "memory"); if (!baddl()) break; });
  if (SLP) __builtin_amdgcn_s_sleep(SLP);
  STAMP(14); STAMPX(4, 10);
  auto ldp5 = [&]() AI {
    P5[0][0] = ld16c(k.ws, k.l16, p5s); P5[0][1] = ld16c(k.ws, k.l16, p5s + 8192);
    P5[1][0] = ld16c(k.ws, k.l16 + 1024, p5s); P5[1][1] = ld16c(k.ws, k.l16 + 1024, p5s + 8192);
  };
  ldp5();
  SB();
  const u32 row = 64 * i + 8 * c + (lane & 7);
  f2 Y[2][4];
  {
    const float fc = blo(cs), fs = blo(ss);
    const u32 yxr = opq(L_YX + (16 * w + (lane >> 3)) * 16);
#pragma unroll
    for (int n = 0; n < 2; ++n) {
      const v4u yx = ldl<v4u>(yxr, n * 128);
#pragma unroll
      for (int j = 0; j < 4; ++j) {
        const f2 da = {bitsf(DL[n][0][j >> 1][2 * (j & 1)]), bitsf(DL[n][0][j >> 1][2 * (j & 1) + 1])}, db = {bitsf(DL[n][1][j >> 1][2 * (j & 1)]), bitsf(DL[n][1][j >> 1][2 * (j & 1) + 1])};
        Y[n][j] = da * fc - db * fs + unpk(row == idx1 ? yx[j] : 0u);
        PIN(Y[n][j]);
      }
    }
  }
  f2 wu2[2][4];
#pragma unroll
  for (int n = 0; n < 2; ++n)
#pragma unroll
    for (int j = 0; j < 4; ++j) { wu2[n][j] = unpk(WV[n][j]); PIN(wu2[n][j]); }
  const u32 sta = opq(L_ST + (lane & 7) * 16);
  SB();
  if (__builtin_expect(__any(umax(max4(P5[1][0]), max4(P5[1][1])) == SENT), 0)) { SPIN { ldp5(); asm volatile("" ::: "memory"); if (!__any(umax(max4(P5[1][0]), max4(P5[1][1])) == SENT)) break; } }
  STAMP(15); STAMPX(4, 11);
  {
    f2 s1 = {0.f, 0.f}, s2 = {0.f, 0.f}, sw = {0.f, 0.f};
#pragma unroll
    for (int n = 0; n < 2; ++n)
#pragma unroll
      for (int j = 0; j < 4; ++j) {
        const f2 v = (unpk(P5[n][0][j]) + Y[n][j]) - unpk(P5[n][1][j]);
        Y[n][j] = v; s1 += v; s2 += v * v; sw += v * wu2[n][j];
      }
    float a = s1[0] + s1[1], bb = s2[0] + s2[1], cc = sw[0] + sw[1];
    a += dppf<0x128>(a); bb += dppf<0x128>(bb); cc += dppf<0x128>(cc);
    auto t0 = __builtin_amdgcn_permlane16_swap(fbits(a), fbits(bb), false, false);
    auto t1 = __builtin_amdgcn_permlane16_swap(fbits(cc), fbits(cc), false, false);
    auto t2 = __builtin_amdgcn_permlane32_swap(fbits(bitsf(t0[0]) + bitsf(t0[1])), fbits(bitsf(t1[0]) + bitsf(t1[1])), false, false);
    stl<float>(sta + w * 128 + q * 4, 0, bitsf(t2[0]) + bitsf(t2[1]));
  }
  bar();
  const f4 st4 = (ldl<f4>(sta) + ldl<f4>(sta, 128)) + (ldl<f4>(sta, 256) + ldl<f4>(sta, 384));
  mean = st4[0] * (1.f / 512); var = fmaxf(st4[1] * (1.f / 512) - mean * mean, 0.f); rstd = __builtin_amdgcn_rsqf(var + 1e-5f);
  const u32 key = mkkey((st4[2] - mean * sumw) * rstd, row);
  u32 best = umax(key, dppu<0x141>(key)); best = umax(best, dppu<0xB1>(best)); best = umax(best, dppu<0x4E>(best));
  if (tid < 8) st4c(k.ws, tid * 2048, O_KEY2 + me * 4, best);
  const bool mine = key == best;
  SB();
  const rsrc_t ro = mkrs(out, 2048 * 1024);
  v4u O[2];
#pragma unroll
  for (int n = 0; n < 2; ++n) {
    v4u z, xx;
#pragma unroll
    for (int j = 0; j < 4; ++j) { const f2 zf = Y[n][j] * rstd - mean * rstd; z[j] = san(pk(zf)); xx[j] = san(pk(Y[n][j])); O[n][j] = pk(Y[n][j] + zf); }
    const u32 cb = (16 * w + 8 * n + (lane >> 3)) * 16;
    if (mine) { st16c(k.ws, cb, O_CAND + me * 2048, z); st16c(k.ws, cb + 1024, O_CAND + me * 2048, xx); }
    PIN(O[n]);
  }
  STAMP(16); STAMPX(4, 12);
  if (w == 0) {
    if (SL1) __builtin_amdgcn_s_sleep(SL1);
    const v4u a = poll16(k.ws, k.l16, O_KEY2 + k.rep * 2048);
    stl<u32>(opq(L_BC), 0, wavemax(max4(a)));
  }
  bar();
  STAMP(17); STAMPX(4, 13);
  const u32 gb = rfl(ldl<u32>(opq(L_BC)));
  const u32 idx2 = 2047u - (gb & 2047u);
  const u32 cso = O_CAND + (idx2 >> 3) * 2048;
  u32 zz = ld4c(k.ws, tid * 4, cso), fz = 0, fx = 0;
  if (red && w == 0) { fz = ld4c(k.ws, (lane & 3) * 4, cso + b * 16); fx = ld4c(k.ws, (lane & 3) * 4 + 1024, cso + b * 16); }
  if (row != idx2) {
#pragma unroll
    for (int n = 0; n < 2; ++n) st16<0>(ro, row * 1024 + (16 * w + 8 * n + (lane >> 3)) * 16, 0, O[n]);
  }
  if (__builtin_expect(__any(zz == SENT), 0)) zz = poll4(k.ws, tid * 4, cso);
  sfor<NG>([&](auto jc) AI { if constexpr (jc == NG - 1) asm volatile("s_nop 3" : "+v"(f.gu)); gemv1_slice<jc>(k, f, zz); });
  bar();
  sfor<NP>([&](auto jc) AI { push_slice<jc>(k, f, vA ? O_YP2 + w * 16 * 7168 + jA * 32 : WS_BYTES); });
  STAMP(18); STAMPX(4, 14);
  if (red) {
    const float sg = sigm(bitsf((gb & 0x80000000u) ? (gb & 0x7ffff800u) | 1024u : ~(gb | 2047u) | 1024u));
    if (SL2) __builtin_amdgcn_s_sleep(SL2);
    yred_poll(k, O_YP2 + b * 7168, YP);
    STAMP(19); STAMPX(4, 15);
    yred_part(k, YP);
    bar();
    if (w == 0) {
      if (__builtin_expect(__any(umax(fz, fx) == SENT), 0)) { fz = poll4(k.ws, (lane & 3) * 4, cso + b * 16); fx = poll4(k.ws, (lane & 3) * 4 + 1024, cso + b * 16); }
      const f2 y = yred_fin(lane & 3, sg);
      const u32 o = pk(unpk(fx) + unpk(pk(unpk(fz) + y)));
      if (lane < 4) __builtin_amdgcn_raw_buffer_store_b32(o, ro, idx2 * 1024 + b * 16 + lane * 4, 0, 0);
    }
  }
  STAMP(20); STAMPX(4, 16);
  asm volatile("fm_code_end:");
}

extern "C" void kernel_launch(void* const* d_in, const int* in_sizes, int n_in, void* d_out, int out_size, void* d_ws, size_t ws_size, hipStream_t stream) {
  (void)hipFuncSetAttribute((const void*)fnet_mod_kernel, hipFuncAttributeMaxDynamicSharedMemorySize, LDS_BYTES);
  fnet_mod_kernel<<<256, 256, LDS_BYTES, stream>>>((const u16*)d_in[0], (const u16*)d_in[1], (const u16*)d_in[2], (const u16*)d_in[3], (const u16*)d_in[4], (const u16*)d_in[6],
                                                   (const u16*)d_in[7], (const u16*)d_in[8], (const u16*)d_in[9], (u16*)d_out, (unsigned char*)d_ws);
}
```
